```python
import torch

def _softmax(x, axis=-1):
    return torch.softmax(x, dim=axis)
def _gelu(x):
    return torch.nn.functional.gelu(x, approximate="tanh")
def _silu(x):
    return torch.nn.functional.silu(x)
def _sigmoid(x):
    return torch.sigmoid(x)
def _layernorm(x, axis=-1, eps=1e-5):
    m = x.mean(dim=axis, keepdim=True)
    v = x.var(dim=axis, keepdim=True, unbiased=False)
    return (x - m) / torch.sqrt(v + eps)
def _scatter_add(tgt, idx, src, axis=0):
    return tgt.clone().index_add_(axis, idx, src)
def _segment_sum(src, seg, n):
    out = torch.zeros((n,) + src.shape[1:], dtype=src.dtype)
    return out.index_add_(0, seg, src)
def _pad(x, pw):
    flat = tuple(v for pair in reversed(pw) for v in pair)
    return torch.nn.functional.pad(x, flat)

kernel_name = 'gated_mix__grouped_kv_mqa__spectral_gfnet__prompt'

def setup_inputs():

    _w = (lambda a: (lambda t: t.to(torch.bfloat16) if t.is_floating_point() else t)(torch.as_tensor(a)))
    def _A_setup():
        B, S, D, H = 1, 2048, 640, 32
        Dh = D // H
        return {
            "x": _w((lambda *s: torch.randn(*s, dtype=torch.bfloat16) * 0.05)(B, S, D)),
            "wq": _w((lambda *s: torch.randn(*s, dtype=torch.bfloat16) * 0.8)(D, H * Dh)),
            "wk": _w((lambda *s: torch.randn(*s, dtype=torch.bfloat16) * 0.8)(D, Dh)),
            "wv": _w((lambda *s: torch.randn(*s, dtype=torch.bfloat16) * 0.05)(D, Dh)),
            "wo": _w((lambda *s: torch.randn(*s, dtype=torch.bfloat16) * 0.05)(H * Dh, D)),
        }
    _A_in = _A_setup()

    def _B_setup():
        B, S, D = 1, 2048, 640
        Df = D // 2 + 1
        return {
            "x": _w((lambda *s: torch.randn(*s, dtype=torch.bfloat16) * 0.05)(B, S, D)),
            "w_r": _w((lambda *s: torch.randn(*s, dtype=torch.bfloat16) * 0.8)(S, Df) * 0.02),
            "w_i": _w((lambda *s: torch.randn(*s, dtype=torch.bfloat16) * 0.8)(S, Df) * 0.02),
        }
    _B_in = _B_setup()

    return {
        "A_x": _A_in["x"],
        "A_wq": _A_in["wq"],
        "A_wk": _A_in["wk"],
        "A_wv": _A_in["wv"],
        "A_wo": _A_in["wo"],
        "B_x": _B_in["x"],
        "B_w_r": _B_in["w_r"],
        "B_w_i": _B_in["w_i"],
        "gate_w": _w((lambda *s: torch.randn(*s, dtype=torch.bfloat16) * 0.05)(640) * 16.0),
    }

def reference(A_x, A_wq, A_wk, A_wv, A_wo, B_x, B_w_r, B_w_i, gate_w):
    def _A_ref(x, wq, wk, wv, wo):
        B, S, D = x.shape
        H = 32
        Dh = D // H
        q = (lambda x, *d: x.permute(*d))((x @ wq).reshape(B, S, H, Dh), 0, 2, 1, 3)
        k = x @ wk
        v = x @ wv
        scores = torch.einsum("bhqd,bkd->bhqk", q, k) * (Dh ** -0.5)
        p = _softmax(scores, axis=-1)
        o = torch.einsum("bhqk,bkd->bhqd", p, v)
        return (lambda x, *d: x.permute(*d))(o, 0, 2, 1, 3).reshape(B, S, H * Dh) @ wo

    def _B_ref(x, w_r, w_i):
        B, S, D = x.shape
        X = (lambda x, axes=(-2,-1): torch.fft.rfft2(x.float(), dim=axes))(x, axes=(-2, -1))
        W = w_r + 1j * w_i
        X = X * W
        y = (lambda x, s, axes=(-2,-1): torch.fft.irfft2(x, s=s, dim=axes).to(torch.bfloat16))(X, (S, D), axes=(-2, -1))
        return y

    x = A_x
    g = _sigmoid((x * gate_w).sum(-1, keepdims=True))
    a = _A_ref(x, A_wq, A_wk, A_wv, A_wo)
    b = _B_ref(x, B_w_r, B_w_i)
    return g * a + (1 - g) * b
```

```cpp
#include <hip/hip_runtime.h>
#include <hip/hip_bf16.h>
#include <stdint.h>

typedef short bf16x8 __attribute__((ext_vector_type(8)));
typedef short s16x4 __attribute__((ext_vector_type(4)));
typedef short s16x2 __attribute__((ext_vector_type(2)));
typedef float f32x16 __attribute__((ext_vector_type(16)));
typedef float f32x4 __attribute__((ext_vector_type(4)));
typedef float f32x2 __attribute__((ext_vector_type(2)));
typedef unsigned u32x4 __attribute__((ext_vector_type(4)));
typedef unsigned u32x2 __attribute__((ext_vector_type(2)));
typedef __bf16 bf2_t __attribute__((ext_vector_type(2)));
typedef unsigned short u16;
#define DI __device__ __forceinline__
#define LAS __attribute__((address_space(3)))

constexpr int S_ = 2048, D_ = 640, NH = 32, DH = 20, NJ = 321;

constexpr double PI_ = 3.14159265358979323846264338327950288;
constexpr double sin_small(double x) { double x2 = x * x, t = x, s = x; for (int i = 1; i < 11; ++i) { t *= -x2 / ((2 * i) * (2 * i + 1)); s += t; } return s; }
constexpr double cos_small(double x) { double x2 = x * x, t = 1, s = 1; for (int i = 1; i < 11; ++i) { t *= -x2 / ((2 * i - 1) * (2 * i)); s += t; } return s; }
constexpr double cos2pi(long m, long n) {
    m %= n; if (m < 0) m += n;
    if (2 * m > n) m = n - m;
    double sg = 1.0;
    m *= 2; n *= 2;
    if (4 * m > n) { m = n / 2 - m; sg = -1.0; }
    double r = (8 * m > n) ? sin_small(2 * PI_ * (double)(n - 4 * m) / (4.0 * (double)n)) : cos_small(2 * PI_ * (double)m / (double)n);
    return sg * r;
}
constexpr double sin2pi(long m, long n) { return cos2pi(4 * m - n, 4 * n); }
constexpr u16 d2bf(double v) { float f = (float)v; uint32_t u = __builtin_bit_cast(uint32_t, f); u += 0x7fffu + ((u >> 16) & 1u); return (u16)(u >> 16); }

struct Base640 { u16 c[640]; u16 ns[640]; };
constexpr Base640 make_base640() { Base640 t{}; for (int m = 0; m < 640; ++m) { t.c[m] = d2bf(cos2pi(m, 640)); t.ns[m] = d2bf(-sin2pi(m, 640)); } return t; }
struct FChunk { u16 v[16 * 640]; };
struct FTable { FChunk c[40]; };
struct B2Tab {
    u16 f64[4][2][3][64][8];
    u16 f32[2][3][64][8];
    float tw1[4][64][16];
    float tw2[4][64][16];
};
#if defined(__HIP_DEVICE_COMPILE__)
constexpr Base640 BASE640 = make_base640();
template <int C> constexpr FChunk make_fchunk() {
    FChunk t{};
    for (int r = 0; r < 16; ++r) {
        const int n = C * 16 + r; int m = n;
        t.v[r * 640 + 0] = BASE640.c[0];
        t.v[r * 640 + 1] = BASE640.c[(n & 1) ? 320 : 0];
        for (int j = 1; j < 320; ++j) { t.v[r * 640 + 2 * j] = BASE640.c[m]; t.v[r * 640 + 2 * j + 1] = BASE640.ns[m]; m += n; if (m >= 640) m -= 640; }
    }
    return t;
}
template <int C> struct FCH { static constexpr FChunk v = make_fchunk<C>(); };
#define FC4(i) FCH<i>::v, FCH<i + 1>::v, FCH<i + 2>::v, FCH<i + 3>::v
struct Base2048 { double c[2048]; };
constexpr Base2048 make_base2048() { Base2048 t{}; for (int m = 0; m < 2048; ++m) t.c[m] = cos2pi(m, 2048); return t; }
constexpr Base2048 BASE2048 = make_base2048();
constexpr double c2048(int m) { return BASE2048.c[m & 2047]; }
constexpr double s2048(int m) { return BASE2048.c[(m - 512) & 2047]; }
constexpr B2Tab make_b2tab() {
    B2Tab t{};
    for (int nt = 0; nt < 4; ++nt) for (int hf = 0; hf < 2; ++hf) for (int l = 0; l < 64; ++l) for (int j = 0; j < 8; ++j) {
        const int s1 = 32 * hf + 8 * (l >> 4) + j, k1 = 16 * nt + (l & 15), m = (s1 * k1 * 32) & 2047;
        t.f64[nt][hf][0][l][j] = d2bf(c2048(m)); t.f64[nt][hf][1][l][j] = d2bf(s2048(m)); t.f64[nt][hf][2][l][j] = d2bf(-s2048(m));
    }
    for (int mt = 0; mt < 2; ++mt) for (int l = 0; l < 64; ++l) for (int j = 0; j < 8; ++j) {
        const int a = 16 * mt + (l & 15), b = 16 * (j >> 2) + 4 * (l >> 4) + (j & 3), m = (a * b * 64) & 2047;
        t.f32[mt][0][l][j] = d2bf(c2048(m)); t.f32[mt][1][l][j] = d2bf(s2048(m)); t.f32[mt][2][l][j] = d2bf(-s2048(m));
    }
    for (int nt = 0; nt < 4; ++nt) for (int l = 0; l < 64; ++l) for (int mt = 0; mt < 2; ++mt) for (int r = 0; r < 4; ++r) {
        { const int s2 = 16 * mt + 4 * (l >> 4) + r, k1 = 16 * nt + (l & 15), m = s2 * k1;
          t.tw1[nt][l][(mt * 4 + r) * 2] = (float)c2048(m); t.tw1[nt][l][(mt * 4 + r) * 2 + 1] = (float)s2048(m); }
        { const int s2 = 16 * mt + (l & 15), k1 = 16 * nt + 4 * (l >> 4) + r, m = s2 * k1;
          t.tw2[nt][l][(mt * 4 + r) * 2] = (float)c2048(m); t.tw2[nt][l][(mt * 4 + r) * 2 + 1] = (float)s2048(m); }
    }
    return t;
}
__device__ const FTable FT = {{FC4(0), FC4(4), FC4(8), FC4(12), FC4(16), FC4(20), FC4(24), FC4(28), FC4(32), FC4(36)}};
__device__ const B2Tab B2T = make_b2tab();
#else
__device__ const FTable FT = {};
__device__ const B2Tab B2T = {};
#endif

constexpr size_t WS_QF  = 0;
constexpr size_t WS_KV  = WS_QF + 2048 * 1536;
constexpr size_t WS_G   = WS_KV + 64 * 3072;
constexpr size_t WS_MX  = WS_G + 2048 * 4;
constexpr size_t WS_X1  = WS_MX + 1024;
constexpr size_t WS_WT  = WS_X1 + 320 * 8192;
constexpr size_t WS_Y1  = WS_WT + 321 * 8192;
constexpr size_t WS_O   = WS_Y1 + 640 * 4096;
constexpr size_t WS_END = WS_O + 2048 * 1280;

DI float bf2f(u16 v) { return __uint_as_float((unsigned)v << 16); }
DI unsigned pk2(float a, float b) { f32x2 v = {a, b}; bf2_t r = __builtin_convertvector(v, bf2_t); return __builtin_bit_cast(unsigned, r); }
DI float lo2f(unsigned u) { return __uint_as_float(u << 16); }
DI float hi2f(unsigned u) { return __uint_as_float(u & 0xffff0000u); }
#define MFMA32(a, b, c) __builtin_amdgcn_mfma_f32_32x32x16_bf16((a), (b), (c), 0, 0, 0)
#define MFMA32K8(a, b, c) __builtin_amdgcn_mfma_f32_32x32x8bf16_1k((a), (b), (c), 0, 0, 0)
#define MFMA16(a, b, c) __builtin_amdgcn_mfma_f32_16x16x32_bf16((a), (b), (c), 0, 0, 0)
typedef short v4i16_t __attribute__((ext_vector_type(4)));
DI s16x4 trd(const LAS char* p) { return __builtin_bit_cast(s16x4, __builtin_amdgcn_ds_read_tr16_b64_v4i16((LAS v4i16_t*)p)); }
DI bf16x8 cat8(s16x4 a, s16x4 b) { return __builtin_shufflevector(a, b, 0, 1, 2, 3, 4, 5, 6, 7); }
DI bf16x8 ldsr128(const LAS char* p) { return *(const LAS bf16x8*)p; }

DI int rswz(int row, int ch) { return row * 128 + ((ch ^ ((row >> 1) & 7)) << 4); }
template <int NROWS, typename RowFn> DI void stage_R(LAS char* dst, const u16* src, int pitch, int k0, RowFn rowidx, int tid, int nthr) {
    for (int u = tid; u < NROWS * 8; u += nthr) { const int row = u >> 3, ch = u & 7;
        const u32x4 v = *(const u32x4*)(src + (size_t)rowidx(row) * pitch + k0 + ch * 8);
        *(LAS u32x4*)(dst + rswz(row, ch)) = v; }
}
DI void stage_T160(LAS char* dst, const u16* src, int pitch, int k0, int col0, int tid, int nthr) {
    for (int u = tid; u < 640; u += nthr) { const int row = u / 10, ch = u % 10;
        const u32x4 v = *(const u32x4*)(src + (size_t)(k0 + row) * pitch + col0 + ch * 8);
        *(LAS u32x4*)(dst + row * 160 + ch * 16) = v; }
}
DI int tswz(int row, int ch) { return row * 128 + ((ch ^ ((((row >> 1) & 1) | (((row >> 3) & 1) << 1)) << 1)) << 4); }
DI void stage_T128(LAS char* dst, const u16* src, int pitch, int k0, int col0, int tid, int nthr) {
    for (int u = tid; u < 512; u += nthr) { const int row = u >> 3, ch = u & 7;
        const u32x4 v = *(const u32x4*)(src + (size_t)(k0 + row) * pitch + col0 + ch * 8);
        *(LAS u32x4*)(dst + tswz(row, ch)) = v; }
}
DI bf16x8 fragR16(const LAS char* img, int row0, int ks, int lane) { return ldsr128(img + rswz(row0 + (lane & 15), ks * 4 + (lane >> 4))); }
DI bf16x8 fragT160_16(const LAS char* img, int col0, int ks, int lane) {
    const int g = lane >> 4, q = (lane & 15) >> 2, p = lane & 3;
    const LAS char* a = img + (32 * ks + 8 * g + q) * 160 + (col0 + 4 * p) * 2;
    return cat8(trd(a), trd(a + 4 * 160));
}
DI bf16x8 fragT128_16(const LAS char* img, int col0, int ks, int lane) {
    const int g = lane >> 4, q = (lane & 15) >> 2, p = lane & 3;
    const int r0 = 32 * ks + 8 * g + q, r1 = r0 + 4, ch = (col0 >> 3) + (p >> 1);
    return cat8(trd(img + tswz(r0, ch) + 8 * (p & 1)), trd(img + tswz(r1, ch) + 8 * (p & 1)));
}

constexpr float QSCALE = 0.22360679774997896f * 1.4426950408889634f;
struct P1 { const u16 *x, *wq, *wk, *wv, *gw, *xb, *wr, *wi; char* ws; };

__global__ __launch_bounds__(512) void k1(P1 p) {
    __shared__ __attribute__((aligned(16))) char smem_[70 * 1024];
    LAS char* smem = (LAS char*)smem_;
    const int tid = threadIdx.x, lane = tid & 63, w = tid >> 6, b = blockIdx.x;
    if (b < 256) {
        const int g = b >> 3, c = b & 7, hh = w >> 1, th = w & 1, r = lane & 31, h = lane >> 5;
        LAS char* xr = smem; LAS char* wt = smem + 8192;
        f32x16 acc = {};
        for (int kc = 0; kc < 10; ++kc) {
            __syncthreads();
            stage_R<64>(xr, p.x, 640, kc * 64, [&](int row) { return 64 * g + row; }, tid, 512);
            stage_T160(wt, p.wq, 640, kc * 64, 80 * c, tid, 512);
            __syncthreads();
#pragma unroll
            for (int ks = 0; ks < 4; ++ks) {
                const bf16x8 bq = ldsr128(xr + rswz(32 * th + r, 2 * ks + h));
                const int blk = (lane >> 4) & 1, q = (lane & 15) >> 2, pp = lane & 3;
                const LAS char* a = wt + (16 * ks + 8 * h + q) * 160 + (20 * hh + 16 * blk + 4 * pp) * 2;
                const bf16x8 aw = cat8(trd(a), trd(a + 4 * 160));
                acc = MFMA32(aw, bq, acc);
            }
        }
        char* qf = p.ws + WS_QF + (size_t)((2 * g + th) * 32 + 4 * c + hh) * 1536;
        u32x4 qa = {pk2(acc[0] * QSCALE, acc[1] * QSCALE), pk2(acc[2] * QSCALE, acc[3] * QSCALE), pk2(acc[4] * QSCALE, acc[5] * QSCALE), pk2(acc[6] * QSCALE, acc[7] * QSCALE)};
        u32x2 qb = {pk2(acc[8] * QSCALE, acc[9] * QSCALE), pk2(acc[10] * QSCALE, acc[11] * QSCALE)};
        if (h) qb = (u32x2){0u, 0u};
        *(u32x4*)(qf + lane * 16) = qa; *(u32x2*)(qf + 1024 + lane * 8) = qb;
    } else if (b < 512) {
        const int s2 = (b - 256) >> 3, ct = (b - 256) & 7, mt = w & 3, ng = w >> 2;
        LAS char* xr = smem; LAS char* wt = smem + 8192;
        f32x4 acc[3] = {};
        const u16* F = (const u16*)&FT;
        for (int kc = 0; kc < 10; ++kc) {
            __syncthreads();
            stage_R<64>(xr, p.xb, 640, kc * 64, [&](int row) { return 32 * row + s2; }, tid, 512);
            stage_T160(wt, F, 640, kc * 64, 80 * ct, tid, 512);
            __syncthreads();
#pragma unroll
            for (int ks = 0; ks < 2; ++ks) {
                const bf16x8 a = fragR16(xr, 16 * mt, ks, lane);
#pragma unroll
                for (int i = 0; i < 3; ++i) if (ng == 0 || i < 2) acc[i] = MFMA16(a, fragT160_16(wt, 16 * (3 * ng + i), ks, lane), acc[i]);
            }
        }
        u16* X1 = (u16*)(p.ws + WS_X1);
#pragma unroll
        for (int i = 0; i < 3; ++i) if (ng == 0 || i < 2) {
            const int c = 80 * ct + 16 * (3 * ng + i) + (lane & 15), cp = c >> 1, part = c & 1, s1 = 16 * mt + 4 * (lane >> 4);
            *(u32x2*)(X1 + (size_t)cp * 4096 + s2 * 128 + part * 64 + s1) = (u32x2){pk2(acc[i][0], acc[i][1]), pk2(acc[i][2], acc[i][3])};
        }
    } else if (b < 544) {
        const int g = b - 512, mt = w & 3, role = w >> 2;
        LAS char* xr = smem; LAS char* wt = smem + 8192;
        f32x4 acc[2] = {};
        for (int kc = 0; kc < 10; ++kc) {
            __syncthreads();
            stage_R<64>(xr, p.x, 640, kc * 64, [&](int row) { return 64 * g + row; }, tid, 512);
            for (int u = tid; u < 64 * 80; u += 512) { const int row = u / 80, col = u % 80, k = kc * 64 + row; u16 v = 0;
                if (col < 20) v = p.wk[k * 20 + col]; else if (col == 20) v = p.gw[k]; else if (col >= 32 && col < 52) v = p.wv[k * 20 + col - 32];
                *(LAS u16*)(wt + row * 160 + col * 2) = v; }
            __syncthreads();
#pragma unroll
            for (int ks = 0; ks < 2; ++ks) {
                const bf16x8 xf = fragR16(xr, 16 * mt, ks, lane);
#pragma unroll
                for (int i = 0; i < 2; ++i) {
                    const bf16x8 wf = fragT160_16(wt, 32 * role + 16 * i, ks, lane);
                    acc[i] = role == 0 ? MFMA16(wf, xf, acc[i]) : MFMA16(xf, wf, acc[i]);
                }
            }
        }
        const int gq = lane >> 4, cl = lane & 15;
        const int tile = 2 * g + (mt >> 1);
        char* kv = p.ws + WS_KV + (size_t)tile * 3072;
        LAS float* red = (LAS float*)(smem + 32768);
        float mx;
        if (role == 0) {
            const int rho = 16 * (mt & 1) + cl;
            const unsigned u0 = pk2(acc[0][0], acc[0][1]), u1 = pk2(acc[0][2], acc[0][3]);
            *(u32x2*)(kv + (gq & 1) * 512 + rho * 16 + (gq >> 1) * 8) = (u32x2){u0, u1};
            const unsigned t0 = pk2(acc[1][0], acc[1][1]), t1 = pk2(acc[1][2], acc[1][3]);
            float ss = lo2f(u0) * lo2f(u0) + hi2f(u0) * hi2f(u0) + lo2f(u1) * lo2f(u1) + hi2f(u1) * hi2f(u1);
            if (gq == 0) { *(u32x2*)(kv + 1024 + rho * 8) = (u32x2){t0, t1}; *(u32x2*)(kv + 1280 + rho * 8) = (u32x2){0x00003f80u, 0u};
                ss += lo2f(t0) * lo2f(t0) + hi2f(t0) * hi2f(t0) + lo2f(t1) * lo2f(t1) + hi2f(t1) * hi2f(t1); }
            if (gq == 1) { const float z = acc[1][0]; ((float*)(p.ws + WS_G))[64 * g + 16 * mt + cl] = 1.0f / (1.0f + __expf(-z)); }
            ss += __shfl_xor(ss, 16); ss += __shfl_xor(ss, 32);
            mx = ss;
        } else {
            const int s = mt & 1, hh = gq & 1, jo = gq >> 1;
            const unsigned a0 = pk2(acc[0][0], acc[0][1]), a1 = pk2(acc[0][2], acc[0][3]), b0 = pk2(acc[1][0], acc[1][1]), b1 = pk2(acc[1][2], acc[1][3]);
            char* vb = kv + 1536 + (s * 2 + hh) * 336 + jo * 8;
            *(u32x2*)(vb + cl * 16) = (u32x2){a0, a1};
            if (cl < 4) *(u32x2*)(vb + (16 + cl) * 16) = (u32x2){b0, b1};
            if (cl == 4) *(u32x2*)(vb + 20 * 16) = (u32x2){0x3f803f80u, 0x3f803f80u};
            mx = fmaxf(fmaxf(fabsf(lo2f(a0)), fabsf(hi2f(a0))), fmaxf(fabsf(lo2f(a1)), fabsf(hi2f(a1))));
            if (cl < 4) mx = fmaxf(mx, fmaxf(fmaxf(fabsf(lo2f(b0)), fabsf(hi2f(b0))), fmaxf(fabsf(lo2f(b1)), fabsf(hi2f(b1)))));
        }
        for (int o = 1; o < 64; o <<= 1) mx = fmaxf(mx, __shfl_xor(mx, o));
        __syncthreads();
        if (lane == 0) red[w] = mx;
        __syncthreads();
        if (tid == 0) { float* MX = (float*)(p.ws + WS_MX);
            MX[g] = fmaxf(fmaxf(red[0], red[1]), fmaxf(red[2], red[3])); MX[32 + g] = fmaxf(fmaxf(red[4], red[5]), fmaxf(red[6], red[7])); }
    } else {
        const int bb = b - 544, jb = bb >> 3, q = bb & 7;
        LAS u16* t = (LAS u16*)smem;
        const int nj = (jb == 5) ? 1 : 64;
        for (int u = tid; u < 2 * 256 * 64; u += 512) { const int jj = u & 63, kk = (u >> 6) & 255, a = u >> 14;
            if (jj < nj) t[(a * 256 + kk) * 66 + jj] = (a ? p.wi : p.wr)[(size_t)(256 * q + kk) * NJ + 64 * jb + jj]; }
        __syncthreads();
        u16* WT = (u16*)(p.ws + WS_WT);
        for (int u = tid; u < 64 * 64; u += 512) { const int k1 = u & 63, jj = u >> 6; if (jj >= nj) continue;
            u16 v[8];
#pragma unroll
            for (int r = 0; r < 4; ++r) { v[2 * r] = t[(64 * r + k1) * 66 + jj]; v[2 * r + 1] = t[(256 + 64 * r + k1) * 66 + jj]; }
            const int mt2 = q >> 2, gq = q & 3, nt = k1 >> 4, ln = gq * 16 + (k1 & 15);
            u32x4 o = {v[0] | ((unsigned)v[1] << 16), v[2] | ((unsigned)v[3] << 16), v[4] | ((unsigned)v[5] << 16), v[6] | ((unsigned)v[7] << 16)};
            *(u32x4*)(WT + (size_t)(64 * jb + jj) * 4096 + (mt2 * 4 + nt) * 512 + ln * 8) = o; }
    }
}

DI void cmul(float& re, float& im, float c, float s) { const float a = re, b = im; re = a * c - b * s; im = a * s + b * c; }
DI bf16x8 pk8(const f32x4& a, const f32x4& b) { u32x4 u = {pk2(a[0], a[1]), pk2(a[2], a[3]), pk2(b[0], b[1]), pk2(b[2], b[3])}; return __builtin_bit_cast(bf16x8, u); }
DI void b2_column(char* ws, int j, int nt, int lane, LAS char* xch, bool active) {
    const int g = lane >> 4, cl = lane & 15;
    const bool special = (j == 0 || j == 320);
    const int cp = special ? 0 : j;
    const char* X1 = ws + WS_X1 + (size_t)cp * 8192;
    const bf16x8* f64 = (const bf16x8*)&B2T.f64[nt][0][0][0][0];
    const bf16x8* f32 = (const bf16x8*)&B2T.f32[0][0][0][0];
    f32x4 ure[2], uim[2];
    if (active) {
        bf16x8 F64[2][3], F32[2][3];
#pragma unroll
        for (int a = 0; a < 2; ++a)
#pragma unroll
            for (int t = 0; t < 3; ++t) { F64[a][t] = f64[(a * 3 + t) * 64 + lane]; F32[a][t] = f32[(a * 3 + t) * 64 + lane]; }
        f32x4 tre[2] = {}, tim[2] = {};
#pragma unroll
        for (int mt = 0; mt < 2; ++mt)
#pragma unroll
            for (int hf = 0; hf < 2; ++hf) {
                const char* a = X1 + (16 * mt + cl) * 256 + (32 * hf + 8 * g) * 2;
                bf16x8 zr = *(const bf16x8*)(a + (j == 320 ? 128 : 0));
                bf16x8 zi = *(const bf16x8*)(a + 128);
                if (special) zi = (bf16x8){0, 0, 0, 0, 0, 0, 0, 0};
                tre[mt] = MFMA16(zr, F64[hf][0], tre[mt]); tre[mt] = MFMA16(zi, F64[hf][1], tre[mt]);
                tim[mt] = MFMA16(zi, F64[hf][0], tim[mt]); tim[mt] = MFMA16(zr, F64[hf][2], tim[mt]);
            }
        const float* tw1 = &B2T.tw1[nt][lane][0];
#pragma unroll
        for (int mt = 0; mt < 2; ++mt)
#pragma unroll
            for (int r = 0; r < 4; ++r) { float a = tre[mt][r], bb = tim[mt][r]; cmul(a, bb, tw1[(mt * 4 + r) * 2], -tw1[(mt * 4 + r) * 2 + 1]); tre[mt][r] = a; tim[mt][r] = bb; }
        const bf16x8 br = pk8(tre[0], tre[1]), bi = pk8(tim[0], tim[1]);
        f32x4 zre[2], zim[2];
#pragma unroll
        for (int mt = 0; mt < 2; ++mt) {
            zre[mt] = MFMA16(F32[mt][0], br, ((f32x4){0, 0, 0, 0})); zre[mt] = MFMA16(F32[mt][1], bi, zre[mt]);
            zim[mt] = MFMA16(F32[mt][0], bi, ((f32x4){0, 0, 0, 0})); zim[mt] = MFMA16(F32[mt][2], br, zim[mt]);
        }
        const float sc = (special ? 1.0f : 2.0f) / (2048.0f * 640.0f);
        const char* WT = ws + WS_WT + (size_t)j * 8192;
#pragma unroll
        for (int mt = 0; mt < 2; ++mt) {
            const u32x4 wv = *(const u32x4*)(WT + (mt * 4 + nt) * 1024 + lane * 16);
#pragma unroll
            for (int r = 0; r < 4; ++r) { float a = zre[mt][r], bb = zim[mt][r]; cmul(a, bb, lo2f(wv[r]) * sc, hi2f(wv[r]) * sc); zre[mt][r] = a; zim[mt][r] = bb; }
        }
        const bf16x8 ar = pk8(zre[0], zre[1]), ai = pk8(zim[0], zim[1]);
#pragma unroll
        for (int n3 = 0; n3 < 2; ++n3) {
            ure[n3] = MFMA16(ar, F32[n3][0], ((f32x4){0, 0, 0, 0})); ure[n3] = MFMA16(ai, F32[n3][2], ure[n3]);
            uim[n3] = MFMA16(ai, F32[n3][0], ((f32x4){0, 0, 0, 0})); uim[n3] = MFMA16(ar, F32[n3][1], uim[n3]);
        }
        const float* tw2 = &B2T.tw2[nt][lane][0];
#pragma unroll
        for (int n3 = 0; n3 < 2; ++n3) {
#pragma unroll
            for (int r = 0; r < 4; ++r) { float a = ure[n3][r], bb = uim[n3][r]; cmul(a, bb, tw2[(n3 * 4 + r) * 2], tw2[(n3 * 4 + r) * 2 + 1]); ure[n3][r] = a; uim[n3][r] = bb; }
            LAS char* d = xch + (16 * n3 + cl) * 256 + (16 * nt + 4 * g) * 2;
            *(LAS u32x2*)d = (u32x2){pk2(ure[n3][0], ure[n3][1]), pk2(ure[n3][2], ure[n3][3])};
            *(LAS u32x2*)(d + 128) = (u32x2){pk2(uim[n3][0], uim[n3][1]), pk2(uim[n3][2], uim[n3][3])};
        }
    }
    __syncthreads();
    if (active) {
        bf16x8 F64[2][3];
#pragma unroll
        for (int a = 0; a < 2; ++a)
#pragma unroll
            for (int t = 0; t < 3; ++t) F64[a][t] = f64[(a * 3 + t) * 64 + lane];
        u16* Y1 = (u16*)(ws + WS_Y1);
#pragma unroll
        for (int mt = 0; mt < 2; ++mt) {
            f32x4 yre = {}, yim = {};
#pragma unroll
            for (int hf = 0; hf < 2; ++hf) {
                const LAS char* a = xch + (16 * mt + cl) * 256 + (32 * hf + 8 * g) * 2;
                const bf16x8 ur = ldsr128(a), ui = ldsr128(a + 128);
                yre = MFMA16(ur, F64[hf][0], yre); yre = MFMA16(ui, F64[hf][2], yre);
                yim = MFMA16(ui, F64[hf][0], yim); yim = MFMA16(ur, F64[hf][1], yim);
            }
            const int tok = 32 * (16 * nt + cl) + 16 * mt + 4 * g;
            const int crow = special ? (j == 0 ? 0 : 1) : 2 * j;
            *(u32x2*)(Y1 + (size_t)crow * 2048 + tok) = (u32x2){pk2(yre[0], yre[1]), pk2(yre[2], yre[3])};
            if (!special) *(u32x2*)(Y1 + (size_t)(crow + 1) * 2048 + tok) = (u32x2){pk2(yim[0], yim[1]), pk2(yim[2], yim[3])};
        }
    }
}

constexpr int TPC = 8;
constexpr int CHB = TPC * 3072;
constexpr float MBOUND = 40.0f;
DI void glds16(const void* g, unsigned lds_base) {
    unsigned sv; asm volatile("s_mov_b32 %0, m0\n\ts_mov_b32 m0, %2\n\ts_nop 0\n\tglobal_load_lds_dwordx4 %1, off\n\ts_mov_b32 m0, %0" : "=&s"(sv) : "v"(g), "s"(lds_base) : "memory"); }
#define SBAR() __builtin_amdgcn_sched_barrier(0)
#define PIN(x) asm volatile("" : "+v"(x))

struct P2 { char* ws; };
__global__ __launch_bounds__(512) void k2(P2 p) {
    extern __shared__ __attribute__((aligned(16))) char smem_[];
    LAS char* smem = (LAS char*)smem_;
    const int tid = threadIdx.x, lane = tid & 63, w = __builtin_amdgcn_readfirstlane(tid >> 6), b = blockIdx.x;
    if (b >= 256) {
        const int j = 2 * (b - 256) + (w >> 2);
        b2_column(p.ws, j, w & 3, lane, smem + (w >> 2) * 8192, j < NJ);
        return;
    }
    const int g = b >> 3, c = b & 7, r = lane & 31, h = lane >> 5;
    const int head = 4 * c + (w >> 1), th = w & 1;
    const char* kvg = p.ws + WS_KV;
    const unsigned lds0 = (unsigned)(uintptr_t)smem_;
#define DMA_CHUNK(ch, slot) do { _Pragma("unroll") for (int i_ = 0; i_ < 3; ++i_) \
        glds16(kvg + (size_t)(ch) * CHB + (w * 3 + i_) * 1024 + lane * 16, (unsigned)__builtin_amdgcn_readfirstlane(lds0 + (slot) * CHB + (w * 3 + i_) * 1024)); } while (0)
    DMA_CHUNK(0, 0); DMA_CHUNK(1, 1);
    const char* qf = p.ws + WS_QF + (size_t)((2 * g + th) * 32 + head) * 1536;
    u32x4 qa_u = *(const u32x4*)(qf + lane * 16);
    u32x2 qb_u = *(const u32x2*)(qf + 1024 + lane * 8);
    const float* MX = (const float*)(p.ws + WS_MX);
    float kmx = MX[lane & 31], vmx = MX[32 + (lane & 31)];
#pragma unroll
    for (int o = 1; o < 32; o <<= 1) { kmx = fmaxf(kmx, __shfl_xor(kmx, o)); vmx = fmaxf(vmx, __shfl_xor(vmx, o)); }
    float qq = 0.f;
#pragma unroll
    for (int i = 0; i < 4; ++i) qq += lo2f(qa_u[i]) * lo2f(qa_u[i]) + hi2f(qa_u[i]) * hi2f(qa_u[i]);
#pragma unroll
    for (int i = 0; i < 2; ++i) qq += lo2f(qb_u[i]) * lo2f(qb_u[i]) + hi2f(qb_u[i]) * hi2f(qb_u[i]);
    qq += __shfl_xor(qq, 32);
    const float mb = sqrtf(qq * kmx) * 1.01f + 1e-6f;
    const bool fast = __all(mb <= MBOUND) && vmx <= 1.0e9f && (vmx >= 1.0e-9f || vmx == 0.0f);
    if (fast && h) qb_u = (u32x2){pk2(-mb, 0.f) , 0u};
    const bf16x8 qa = __builtin_bit_cast(bf16x8, qa_u);
    const s16x4 qb = __builtin_bit_cast(s16x4, qb_u);
    const int koffA = h * 512 + r * 16, koffB = 1024 + h * 256 + r * 8;
    const int vr = r <= 20 ? r : (r < 28 ? 20 : r - 12);
    const int voff = 1536 + h * 336 + vr * 16;
    f32x16 o = {};
    f32x16 zero16 = {};
    asm volatile("s_waitcnt vmcnt(3)\n\ts_barrier" ::: "memory");
    if (fast) {
        bf16x8 ka = ldsr128(smem + koffA); s16x4 kb = *(const LAS s16x4*)(smem + koffB);
        f32x16 sc = MFMA32(ka, qa, zero16); sc = MFMA32K8(kb, qb, sc);
        ka = ldsr128(smem + 3072 + koffA); kb = *(const LAS s16x4*)(smem + 3072 + koffB);
        bf16x8 v0 = ldsr128(smem + voff), v1 = ldsr128(smem + voff + 672);
        int slot = 0;
        for (int ch = 0; ch < 64 / TPC; ++ch) {
            const int nslot = slot == 2 ? 0 : slot + 1;
            const LAS char* cur = smem + slot * CHB; const LAS char* nxt = smem + nslot * CHB;
#pragma unroll
            for (int t = 0; t < TPC; ++t) {
                if (t == TPC - 2) {
                    asm volatile("s_waitcnt vmcnt(0) lgkmcnt(0)\n\ts_barrier" ::: "memory");
                    if (ch + 2 < 64 / TPC) { const int fs = nslot == 2 ? 0 : nslot + 1; DMA_CHUNK(ch + 2, fs); }
                }
                f32x16 sn = MFMA32(ka, qa, zero16); sn = MFMA32K8(kb, qb, sn);
                { const LAS char* kp = (t + 2 < TPC) ? cur + (t + 2) * 3072 : nxt + (t + 2 - TPC) * 3072;
                  ka = ldsr128(kp + koffA); kb = *(const LAS s16x4*)(kp + koffB); }
                u32x4 p0, p1;
#pragma unroll
                for (int i = 0; i < 4; ++i) p0[i] = pk2(__builtin_amdgcn_exp2f(sc[2 * i]), __builtin_amdgcn_exp2f(sc[2 * i + 1]));
#pragma unroll
                for (int i = 0; i < 4; ++i) p1[i] = pk2(__builtin_amdgcn_exp2f(sc[8 + 2 * i]), __builtin_amdgcn_exp2f(sc[8 + 2 * i + 1]));
                o = MFMA32(v0, __builtin_bit_cast(bf16x8, p0), o);
                o = MFMA32(v1, __builtin_bit_cast(bf16x8, p1), o);
                { const LAS char* vp = (t + 1 < TPC) ? cur + (t + 1) * 3072 : nxt;
                  v0 = ldsr128(vp + voff); v1 = ldsr128(vp + voff + 672); }
                sc = sn;
            }
            slot = nslot;
        }
    } else {
        float m = -INFINITY;
        int slot = 0;
        for (int ch = 0; ch < 64 / TPC; ++ch) {
            const int nslot = slot == 2 ? 0 : slot + 1;
            for (int t = 0; t < TPC; ++t) {
                if (t == TPC - 2) {
                    asm volatile("s_waitcnt vmcnt(0) lgkmcnt(0)\n\ts_barrier" ::: "memory");
                    if (ch + 2 < 64 / TPC) { const int fs = nslot == 2 ? 0 : nslot + 1; DMA_CHUNK(ch + 2, fs); }
                }
                const LAS char* tp = smem + slot * CHB + t * 3072;
                const bf16x8 ka = ldsr128(tp + koffA); const s16x4 kb = *(const LAS s16x4*)(tp + koffB);
                f32x16 sc = MFMA32(ka, qa, zero16); sc = MFMA32K8(kb, qb, sc);
                float tm = sc[0];
#pragma unroll
                for (int i = 1; i < 16; ++i) tm = fmaxf(tm, sc[i]);
                tm = fmaxf(tm, __shfl_xor(tm, 32));
                const float mn = fmaxf(m, tm), al = (m == -INFINITY) ? 0.f : __builtin_amdgcn_exp2f(m - mn);
                m = mn;
                u32x4 p0, p1;
#pragma unroll
                for (int i = 0; i < 4; ++i) { p0[i] = pk2(__builtin_amdgcn_exp2f(sc[2 * i] - mn), __builtin_amdgcn_exp2f(sc[2 * i + 1] - mn));
                    p1[i] = pk2(__builtin_amdgcn_exp2f(sc[8 + 2 * i] - mn), __builtin_amdgcn_exp2f(sc[8 + 2 * i + 1] - mn)); }
#pragma unroll
                for (int i = 0; i < 16; ++i) o[i] *= al;
                const bf16x8 v0 = ldsr128(tp + voff), v1 = ldsr128(tp + voff + 672);
                o = MFMA32(v0, __builtin_bit_cast(bf16x8, p0), o);
                o = MFMA32(v1, __builtin_bit_cast(bf16x8, p1), o);
            }
            slot = nslot;
        }
    }
    const float l = __shfl(o[8], r + 32);
    const float il = 1.0f / l;
    u16* O = (u16*)(p.ws + WS_O) + (size_t)(64 * g + 32 * th + r) * 640 + head * 20;
    *(u32x2*)(O + 4 * h) = (u32x2){pk2(o[0] * il, o[1] * il), pk2(o[2] * il, o[3] * il)};
    *(u32x2*)(O + 8 + 4 * h) = (u32x2){pk2(o[4] * il, o[5] * il), pk2(o[6] * il, o[7] * il)};
    if (!h) *(u32x2*)(O + 16) = (u32x2){pk2(o[8] * il, o[9] * il), pk2(o[10] * il, o[11] * il)};
}

struct P3 { const u16* wo; char* ws; u16* out; };
__global__ __launch_bounds__(512) void k3(P3 p) {
    __shared__ __attribute__((aligned(16))) char smem_[40 * 1024];
    LAS char* smem = (LAS char*)smem_;
    const int tid = threadIdx.x, lane = tid & 63, w = tid >> 6, b = blockIdx.x;
    const int g = b >> 3, c = b & 7, nt = w & 3, mg = w >> 2;
    LAS char* orr = smem; LAS char* wt = smem + 8192; LAS char* fr = smem + 8192 + 10240; LAS char* yt = fr + 10240;
    f32x4 aa[3] = {}, ab[3] = {};
    const u16* O = (const u16*)(p.ws + WS_O); const u16* Y1 = (const u16*)(p.ws + WS_Y1); const u16* F = (const u16*)&FT;
    for (int kc = 0; kc < 10; ++kc) {
        __syncthreads();
        stage_R<64>(orr, O, 640, kc * 64, [&](int row) { return 64 * g + row; }, tid, 512);
        stage_T160(wt, p.wo, 640, kc * 64, 80 * c, tid, 512);
        stage_R<80>(fr, F, 640, kc * 64, [&](int row) { return 80 * c + row; }, tid, 512);
        stage_T128(yt, Y1, 2048, kc * 64, 64 * g, tid, 512);
        __syncthreads();
#pragma unroll
        for (int ks = 0; ks < 2; ++ks) {
            const bf16x8 bo = fragR16(orr, 16 * nt, ks, lane), by = fragT128_16(yt, 16 * nt, ks, lane);
#pragma unroll
            for (int i = 0; i < 3; ++i) if (mg == 0 || i < 2) {
                aa[i] = MFMA16(fragT160_16(wt, 16 * (3 * mg + i), ks, lane), bo, aa[i]);
                ab[i] = MFMA16(fragR16(fr, 16 * (3 * mg + i), ks, lane), by, ab[i]);
            }
        }
    }
    const int tok = 64 * g + 16 * nt + (lane & 15);
    const float gt = ((const float*)(p.ws + WS_G))[tok];
#pragma unroll
    for (int i = 0; i < 3; ++i) if (mg == 0 || i < 2) {
        const int n = 80 * c + 16 * (3 * mg + i) + 4 * (lane >> 4);
        float v[4];
#pragma unroll
        for (int e = 0; e < 4; ++e) v[e] = gt * aa[i][e] + (1.0f - gt) * ab[i][e];
        *(u32x2*)(p.out + (size_t)tok * 640 + n) = (u32x2){pk2(v[0], v[1]), pk2(v[2], v[3])};
    }
}

extern "C" void kernel_launch(void* const* d_in, const int* in_sizes, int n_in, void* d_out, int out_size, void* d_ws, size_t ws_size, hipStream_t stream) {
    P1 p1; p1.x = (const u16*)d_in[0]; p1.wq = (const u16*)d_in[1]; p1.wk = (const u16*)d_in[2]; p1.wv = (const u16*)d_in[3];
    p1.gw = (const u16*)d_in[8]; p1.xb = (const u16*)d_in[0]; p1.wr = (const u16*)d_in[6]; p1.wi = (const u16*)d_in[7]; p1.ws = (char*)d_ws;
    k1<<<544 + 48, 512, 0, stream>>>(p1);
    P2 p2; p2.ws = (char*)d_ws;
    hipFuncSetAttribute((const void*)k2, hipFuncAttributeMaxDynamicSharedMemorySize, 3 * CHB);
    k2<<<256 + 161, 512, 3 * CHB, stream>>>(p2);
    P3 p3; p3.wo = (const u16*)d_in[4]; p3.ws = (char*)d_ws; p3.out = (u16*)d_out;
    k3<<<256, 512, 0, stream>>>(p3);
}
```

```cpp
#include <hip/hip_runtime.h>
#include <hip/hip_bf16.h>
#include <stdint.h>

typedef short bf16x8 __attribute__((ext_vector_type(8)));
typedef short s16x4 __attribute__((ext_vector_type(4)));
typedef float f32x16 __attribute__((ext_vector_type(16)));
typedef float f32x4 __attribute__((ext_vector_type(4)));
typedef float f32x2 __attribute__((ext_vector_type(2)));
typedef unsigned u32x4 __attribute__((ext_vector_type(4)));
typedef unsigned u32x2 __attribute__((ext_vector_type(2)));
typedef __bf16 bf2_t __attribute__((ext_vector_type(2)));
typedef unsigned short u16;
#define DI __device__ __forceinline__
#define LAS __attribute__((address_space(3)))

constexpr int NJ = 321;

constexpr double PI_ = 3.14159265358979323846264338327950288;
constexpr double sin_small(double x) { double x2 = x * x, t = x, s = x; for (int i = 1; i < 11; ++i) { t *= -x2 / ((2 * i) * (2 * i + 1)); s += t; } return s; }
constexpr double cos_small(double x) { double x2 = x * x, t = 1, s = 1; for (int i = 1; i < 11; ++i) { t *= -x2 / ((2 * i - 1) * (2 * i)); s += t; } return s; }
constexpr double cos2pi(long m, long n) {
    m %= n; if (m < 0) m += n;
    if (2 * m > n) m = n - m;
    double sg = 1.0;
    m *= 2; n *= 2;
    if (4 * m > n) { m = n / 2 - m; sg = -1.0; }
    double r = (8 * m > n) ? sin_small(2 * PI_ * (double)(n - 4 * m) / (4.0 * (double)n)) : cos_small(2 * PI_ * (double)m / (double)n);
    return sg * r;
}
constexpr double sin2pi(long m, long n) { return cos2pi(4 * m - n, 4 * n); }
constexpr u16 d2bf(double v) { float f = (float)v; uint32_t u = __builtin_bit_cast(uint32_t, f); u += 0x7fffu + ((u >> 16) & 1u); return (u16)(u >> 16); }

struct B2Tab {
    u16 f64[4][2][3][64][8];
    u16 f32[2][3][64][8];
    float tw1[4][4][64][4];
    float tw2[4][4][64][4];
    u16 d20[2][64][8];
    float twr[4][64][4];
};
#if defined(__HIP_DEVICE_COMPILE__)
struct Base2048 { double c[2048]; };
constexpr Base2048 make_base2048() { Base2048 t{}; for (int m = 0; m < 2048; ++m) t.c[m] = cos2pi(m, 2048); return t; }
constexpr Base2048 BASE2048 = make_base2048();
constexpr double c2048(int m) { return BASE2048.c[m & 2047]; }
constexpr double s2048(int m) { return BASE2048.c[(m - 512) & 2047]; }
constexpr B2Tab make_b2tab() {
    B2Tab t{};
    for (int nt = 0; nt < 4; ++nt) for (int hf = 0; hf < 2; ++hf) for (int l = 0; l < 64; ++l) for (int j = 0; j < 8; ++j) {
        const int s1 = 32 * hf + 8 * (l >> 4) + j, k1 = 16 * nt + (l & 15), m = (s1 * k1 * 32) & 2047;
        t.f64[nt][hf][0][l][j] = d2bf(0.5 * (c2048(m) - s2048(m))); t.f64[nt][hf][1][l][j] = d2bf(0.5 * (c2048(m) + s2048(m))); t.f64[nt][hf][2][l][j] = d2bf(-0.5 * (c2048(m) + s2048(m)));
    }
    for (int mt = 0; mt < 2; ++mt) for (int l = 0; l < 64; ++l) for (int j = 0; j < 8; ++j) {
        const int a = 16 * mt + (l & 15), b = 16 * (j >> 2) + 4 * (l >> 4) + (j & 3), m = (a * b * 64) & 2047;
        t.f32[mt][0][l][j] = d2bf(c2048(m)); t.f32[mt][1][l][j] = d2bf(s2048(m)); t.f32[mt][2][l][j] = d2bf(-s2048(m));
    }
    for (int nt = 0; nt < 4; ++nt) for (int l = 0; l < 64; ++l) for (int i = 0; i < 8; ++i) { const int mt = i >> 2, r = i & 3;
        { const int s2 = 16 * mt + 4 * (l >> 4) + r, k1 = 16 * nt + (l & 15), m = s2 * k1;
          t.tw1[nt][i >> 1][l][i & 1] = (float)c2048(m); t.tw1[nt][i >> 1][l][2 + (i & 1)] = (float)s2048(m); }
        { const int s2 = 16 * mt + (l & 15), k1 = 16 * nt + 4 * (l >> 4) + r, m = s2 * k1;
          t.tw2[nt][i >> 1][l][i & 1] = (float)(c2048(m) * (2.0 / (2048.0 * 640.0))); t.tw2[nt][i >> 1][l][2 + (i & 1)] = (float)(s2048(m) * (2.0 / (2048.0 * 640.0))); }
    }
    for (int l = 0; l < 64; ++l) { const int bb = l & 15, g = l >> 4;
        for (int j = 0; j < 8; ++j) { const int n2 = 8 * g + j; const bool on = n2 < 20 && bb <= 10;
            t.d20[0][l][j] = on ? d2bf(cos2pi(n2 * bb, 20)) : (u16)0; t.d20[1][l][j] = on ? d2bf(-sin2pi(n2 * bb, 20)) : (u16)0; }
        for (int i = 0; i < 8; ++i) { const int n1 = 16 * (i >> 2) + 4 * g + (i & 3);
            t.twr[i >> 1][l][i & 1] = (float)cos2pi(n1 * bb, 640); t.twr[i >> 1][l][2 + (i & 1)] = (float)sin2pi(n1 * bb, 640); } }
    return t;
}
__device__ const B2Tab B2T = make_b2tab();
#else
__device__ const B2Tab B2T = {};
#endif

constexpr size_t WS_EPOCH = 0;
#define KVFP 16
constexpr size_t WS_KVF = 4096;
constexpr size_t WS_XF  = WS_KVF + KVFP * 256;
constexpr size_t WS_YF  = WS_XF + 256 * 128;
constexpr size_t WS_BF  = WS_YF + 256 * 128;
constexpr size_t WS_OF  = WS_BF + 256 * 128;
constexpr size_t WS_G   = WS_OF + 256 * 128;
constexpr size_t WS_KV  = WS_G + 8192;
constexpr size_t WS_X1  = WS_KV + 64 * 3072;
constexpr size_t WS_Y1  = WS_X1 + 640 * 4096;
constexpr size_t WS_O   = WS_Y1 + 640 * 4096;
constexpr size_t WS_WT  = WS_O + 2048 * 1280;
constexpr size_t WS_BX  = WS_WT + 321 * 8192;
constexpr size_t WS_END = WS_BX + 2048 * 1280;

constexpr int L_KVR = 0;
constexpr int L_WKV = 0;
__device__ __forceinline__ int kv_slot(int kh, int k) { return (k >> 1) * 24576 + kh * 12288 + (k & 1) * 6144; }
__device__ __forceinline__ int mrg_off(int w) { return (w & 2) ? kv_slot(w >> 2, 2) + (w & 1) * 3072 : (w >> 2) * 18432 + (w & 1) * 3072; }
__device__ __forceinline__ int qx_off(int w) { return kv_slot(w >> 2, 3) + (w & 3) * 1536; }
constexpr int L_X = 49152;
constexpr int L_FR = 131072;
constexpr int L_WO = 49152;
constexpr int L_B2I = L_X, L_B2X = L_X + 16384, L_Y1S = L_X + 32768;
constexpr int L_B3 = L_FR + 20480;
constexpr int L_BH = 24576, L_FIN = 0;
constexpr int L_STG = 6144;
constexpr int L_X1S = L_FR;
constexpr int L_MISC = 161792;
constexpr int L_CNT = L_MISC, L_GATE = L_MISC + 128, L_GW = L_MISC + 384, L_ARR = L_MISC + 1664;
constexpr int L_TOTAL = 163840;
enum { C_ABORT = 0, C_E0 = 1, C_E1 = 2, C_QX = 3, C_KV0 = 5, C_QRD = 9, C_SVC = 7, C_B1 = 8, C_SHR = 449, C_OOK = 450, C_BRD = 454, C_T5 = 456  , C_XP = 451, C_BX = 452, C_PRE = 453, C_PUB = 10, C_KMX = 12, C_VMX = 13, C_KVRDY = 14, C_MX0 = 16, C_MX1 = 17, C_F0 = 18  , C_T1 = 28  , C_T3 = 6, C_T4 = 4, C_T6 = 11, C_WO = 15, C_Y1 = 448   };
__device__ __forceinline__ int wq_slot(int i) { return i == 0 ? 26624 : i == 1 ? 36864 : L_FR + (i - 2) * 10240; }

DI unsigned pk2(float a, float b) { f32x2 v = {a, b}; bf2_t r = __builtin_convertvector(v, bf2_t); return __builtin_bit_cast(unsigned, r); }
DI float lo2f(unsigned u) { return __uint_as_float(u << 16); }
DI float hi2f(unsigned u) { return __uint_as_float(u & 0xffff0000u); }
#define MFMA32(a, b, c) __builtin_amdgcn_mfma_f32_32x32x16_bf16((a), (b), (c), 0, 0, 0)
#define MFMA32K8(a, b, c) __builtin_amdgcn_mfma_f32_32x32x8bf16_1k((a), (b), (c), 0, 0, 0)
#define MFMA16(a, b, c) __builtin_amdgcn_mfma_f32_16x16x32_bf16((a), (b), (c), 0, 0, 0)
#define SBAR() __builtin_amdgcn_sched_barrier(0)
typedef short v4i16_t __attribute__((ext_vector_type(4)));
DI s16x4 trd(const LAS char* p) { return __builtin_bit_cast(s16x4, __builtin_amdgcn_ds_read_tr16_b64_v4i16((LAS v4i16_t*)p)); }
DI bf16x8 cat8(s16x4 a, s16x4 b) { return __builtin_shufflevector(a, b, 0, 1, 2, 3, 4, 5, 6, 7); }
DI bf16x8 ldsr128(const LAS char* p) { return *(const LAS bf16x8*)p; }
DI int rswz(int row, int ch) { return row * 128 + ((ch ^ ((row >> 1) & 7)) << 4); }
DI int tsw(int row) { return (((row >> 1) & 1) | (((row >> 3) & 1) << 1)) << 1; }
DI int tswz(int row, int ch) { return row * 128 + ((ch ^ tsw(row)) << 4); }
DI bf16x8 fragR16(const LAS char* img, int row0, int ks, int lane) { return ldsr128(img + rswz(row0 + (lane & 15), ks * 4 + (lane >> 4))); }
template <int PITCH> DI bf16x8 fragT16(const LAS char* img, int col0, int krow0, int lane) {
    const int g = lane >> 4, q = (lane & 15) >> 2, p = lane & 3;
    const LAS char* a = img + (krow0 + 8 * g + q) * PITCH + (col0 + 4 * p) * 2;
    return cat8(trd(a), trd(a + 4 * PITCH));
}
DI bf16x8 fragT128_16(const LAS char* img, int col0, int ks, int lane) {
    const int g = lane >> 4, q = (lane & 15) >> 2, p = lane & 3;
    const int r0 = 32 * ks + 8 * g + q, r1 = r0 + 4, ch = (col0 >> 3) + (p >> 1);
    return cat8(trd(img + tswz(r0, ch) + 8 * (p & 1)), trd(img + tswz(r1, ch) + 8 * (p & 1)));
}
DI void glds(unsigned voff, const void* sbase, unsigned ldsaddr) {
    asm volatile("s_mov_b32 m0, %2\n\ts_nop 0\n\tglobal_load_lds_dwordx4 %0, %1" :: "v"(voff), "s"(sbase), "s"(ldsaddr) : "memory"); }
DI void glds_sc1(unsigned voff, const void* sbase, unsigned ldsaddr) {
    asm volatile("s_mov_b32 m0, %2\n\ts_nop 0\n\tglobal_load_lds_dwordx4 %0, %1 sc1" :: "v"(voff), "s"(sbase), "s"(ldsaddr) : "memory"); }
#define WAITV(n) asm volatile("s_waitcnt vmcnt(" #n ")" ::: "memory")
#define WAITL0() asm volatile("s_waitcnt lgkmcnt(0)" ::: "memory")
#define BAR() asm volatile("s_waitcnt lgkmcnt(0)\n\ts_barrier" ::: "memory")
#define GAS __attribute__((address_space(1)))
typedef GAS unsigned long long gu64;
DI void st_sc1_8(void* p, u32x2 v) { asm volatile("global_store_dwordx2 %0, %1, off sc1\n\ts_nop 1" ::"v"(p), "v"(v) : "memory"); }
DI void st_sc1_4(void* p, unsigned v) { asm volatile("global_store_dword %0, %1, off sc1" ::"v"(p), "v"(v) : "memory"); }
DI void st_sc1_16(void* p, u32x4 v) { asm volatile("global_store_dwordx4 %0, %1, off sc1\n\ts_nop 1" ::"v"(p), "v"(v) : "memory"); }
DI unsigned long long ld_flag(const char* p) { return __hip_atomic_load((gu64*)p, __ATOMIC_RELAXED, __HIP_MEMORY_SCOPE_AGENT); }
DI void st_flag(char* p, unsigned long long v) { __hip_atomic_store((gu64*)p, v, __ATOMIC_RELAXED, __HIP_MEMORY_SCOPE_AGENT); }
typedef volatile LAS unsigned* ldsw_t;
constexpr unsigned CAP_GLB = 1u << 14;
#define KVSLEEP 8
#define POLL_AFTER 4
#define WT_AFTER 2
#define WT_GATE C_KVRDY
#define WT_GATE_N 1u
#define WLOAD 8
#define OFSL 2
#define XDLY 16
#define ODLY 0
#define ORSL 2
#define OCH 24
DI void lds_arrive(ldsw_t w) {
    unsigned long long sv; const unsigned a = (unsigned)(uintptr_t)w, one = 1u;
    asm volatile("s_waitcnt lgkmcnt(0)\n\ts_mov_b64 %0, exec\n\ts_mov_b64 exec, 1\n\tds_add_u32 %1, %2\n\ts_mov_b64 exec, %0" : "=&s"(sv) : "v"(a), "v"(one) : "memory"); }
DI unsigned lds_arrive_ret(ldsw_t w) {
    unsigned long long sv; const unsigned a = (unsigned)(uintptr_t)w, one = 1u; unsigned o;
    asm volatile("s_waitcnt lgkmcnt(0)\n\ts_mov_b64 %0, exec\n\ts_mov_b64 exec, 1\n\tds_add_rtn_u32 %1, %2, %3\n\ts_waitcnt lgkmcnt(0)\n\ts_mov_b64 exec, %0" : "=&s"(sv), "=&v"(o) : "v"(a), "v"(one) : "memory");
    return (unsigned)__builtin_amdgcn_readfirstlane(o); }
template <int SLEEP> DI void lds_spin(ldsw_t C, int idx, unsigned target) {
    const unsigned a = (unsigned)(uintptr_t)(C + idx); unsigned v, sp, sv;
    asm volatile("s_mov_b32 %1, 0\n"
                 "1:\n\tds_read_b32 %0, %3\n\ts_add_u32 %1, %1, 1\n\ts_waitcnt lgkmcnt(0)\n\tv_readfirstlane_b32 %2, %0\n\t"
                 "s_cmp_gt_u32 %1, 0x40000\n\ts_cbranch_scc1 2f\n\ts_cmp_ge_u32 %2, %4\n\ts_cbranch_scc1 2f\n\ts_sleep %5\n\ts_branch 1b\n2:"
                 : "=&v"(v), "=&s"(sp), "=&s"(sv) : "v"(a), "s"(target), "i"(SLEEP) : "memory", "scc"); }
DI void lds_wait(ldsw_t C, int idx, unsigned target) { lds_spin<1>(C, idx, target); }
#define FRESH_LANE() int lane_f = threadIdx.x & 63; asm volatile("" : "+v"(lane_f)); const int lane = lane_f, g = lane >> 4, cl = lane & 15; (void)g; (void)cl
#define SVSL 0
#define SVC_SYNC() do { lds_arrive(C + C_SVC); svc_t += 4; lds_spin<SVSL>(C, C_SVC, svc_t); } while (0)
DI u32x4 ld16_sc1(const char* base, unsigned off) {
    const __amdgpu_buffer_rsrc_t rs = __builtin_amdgcn_make_buffer_rsrc((void*)base, 0, 0x7fffffff, 0x00020000);
    return __builtin_bit_cast(u32x4, __builtin_amdgcn_raw_buffer_load_b128(rs, off, 0, 16)); }
template <int NL, int SLEEP> DI void poll_words(const char* base, const int (&offs)[NL], unsigned long long e, ldsw_t C, unsigned (&pay)[NL]) {
    const unsigned e0 = (unsigned)e, e1 = (unsigned)(e >> 32);
    for (unsigned spins = 0;;) {
        u32x4 v[NL];
#pragma unroll
        for (int i = 0; i < NL; ++i) v[i] = ld16_sc1(base, offs[i] < 0 ? 0 : offs[i]);
        bool ok = true;
#pragma unroll
        for (int i = 0; i < NL; ++i) { ok &= (offs[i] < 0) | (v[i][0] == e0 && v[i][1] == e1); pay[i] = v[i][2]; }
        if (__all(ok)) break;
        __builtin_amdgcn_s_sleep(SLEEP);
        if (++spins > CAP_GLB || __builtin_amdgcn_readfirstlane(C[C_ABORT])) { C[C_ABORT] = 1u; break; }
    }
    asm volatile("" ::: "memory");
}
DI float wave_max_nn(float f) {
    int x = __float_as_int(f);
    x = max(x, __builtin_amdgcn_update_dpp(x, x, 0x128, 0xf, 0xf, false)); x = max(x, __builtin_amdgcn_update_dpp(x, x, 0x124, 0xf, 0xf, false));
    x = max(x, __builtin_amdgcn_update_dpp(x, x, 0x122, 0xf, 0xf, false)); x = max(x, __builtin_amdgcn_update_dpp(x, x, 0x121, 0xf, 0xf, false));
    const int a = __builtin_amdgcn_readlane(x, 0), b = __builtin_amdgcn_readlane(x, 16), c = __builtin_amdgcn_readlane(x, 32), d = __builtin_amdgcn_readlane(x, 48);
    return __int_as_float(max(max(a, b), max(c, d)));
}
DI float sum_xor32(float f) { const auto r = __builtin_amdgcn_permlane32_swap(__float_as_uint(f), __float_as_uint(f), false, false); return __uint_as_float(r[0]) + __uint_as_float(r[1]); }
DI float sum_xor16(float f) { const auto r = __builtin_amdgcn_permlane16_swap(__float_as_uint(f), __float_as_uint(f), false, false); return __uint_as_float(r[0]) + __uint_as_float(r[1]); }

constexpr float QSCALE = 0.22360679774997896f * 1.4426950408889634f;
constexpr float MBOUND = 40.0f;
constexpr int KVCH = 12288;
#define ATTN_ASM_TEXT0 \
    "s_mov_b32 %[sm0], m0\n\t" \
    "ds_read_b128 %[ka], %[ak]\n\t" \
    "ds_read_b64 %[kb], %[akb]\n\t" \
    "ds_read_b128 %[v0], %[av]\n\t" \
    "ds_read_b128 %[v1], %[av] offset:704\n\t" \
    "v_mov_b32 v104, 0\n\t" \
    "v_mov_b32 v105, 0\n\t" \
    "v_mov_b32 v106, 0\n\t" \
    "v_mov_b32 v107, 0\n\t" \
    "v_mov_b32 v108, 0\n\t" \
    "v_mov_b32 v109, 0\n\t" \
    "v_mov_b32 v110, 0\n\t" \
    "v_mov_b32 v111, 0\n\t" \
    "v_mov_b32 v92, 0\n\t" \
    "v_mov_b32 v93, 0\n\t" \
    "v_mov_b32 v94, 0\n\t" \
    "v_mov_b32 v95, 0\n\t" \
    "v_mov_b32 %[vit], 1\n\t" \
    "s_mov_b32 %[sc], 0\n\t" \
    "s_waitcnt lgkmcnt(0)\n\t" \
    "v_mfma_f32_32x32x16_bf16 v[64:79], %[ka], %[qaa], 0\n\t" \
    "s_nop 7\n\t" \
    "s_nop 7\n\t" \
    "v_mfma_f32_32x32x8bf16_1k v[64:79], %[kb], %[qba], v[64:79]\n\t" \
    "s_nop 7\n\t" \
    "s_nop 7\n\t" \
    "1:\n\t" \
    "s_cmp_eq_u32 %[sc], %[slast]\n\t" \
    "ds_write_b32 %[awr], %[vit] offset:0\n\t" \
    "v_mfma_f32_32x32x16_bf16 %[ob], %[v0], v[104:107], %[ob]\n\t" \
    "ds_read_b128 %[v0], %[av] offset:0\n\t" \
    "v_cvt_pk_bf16_f32 v110, v92, v93\n\t" \
    "v_cvt_pk_bf16_f32 v111, v94, v95\n\t" \
    "v_exp_f32 v64, v64\n\t" \
    "v_exp_f32 v65, v65\n\t" \
    "v_exp_f32 v66, v66\n\t" \
    "v_exp_f32 v67, v67\n\t" \
    "v_mfma_f32_32x32x16_bf16 v[80:95], %[ka], %[qab], 0\n\t" \
    "ds_read_b128 %[ka], %[ak] offset:3072\n\t" \
    "v_cvt_pk_bf16_f32 v96, v64, v65\n\t" \
    "v_cvt_pk_bf16_f32 v97, v66, v67\n\t" \
    "v_exp_f32 v68, v68\n\t" \
    "v_exp_f32 v69, v69\n\t" \
    "v_exp_f32 v70, v70\n\t" \
    "v_exp_f32 v71, v71\n\t" \
    "v_mfma_f32_32x32x8bf16_1k v[80:95], %[kb], %[qbb], v[80:95]\n\t" \
    "ds_read_b64 %[kb], %[akb] offset:3072\n\t" \
    "v_cvt_pk_bf16_f32 v98, v68, v69\n\t" \
    "v_cvt_pk_bf16_f32 v99, v70, v71\n\t" \
    "v_exp_f32 v72, v72\n\t" \
    "v_exp_f32 v73, v73\n\t" \
    "v_exp_f32 v74, v74\n\t" \
    "v_exp_f32 v75, v75\n\t" \
    "s_waitcnt lgkmcnt(2)\n\t" \
    "v_mfma_f32_32x32x16_bf16 %[oa], %[v0], v[96:99], %[oa]\n\t" \
    "v_cvt_pk_bf16_f32 v100, v72, v73\n\t" \
    "v_cvt_pk_bf16_f32 v101, v74, v75\n\t" \
    "v_exp_f32 v76, v76\n\t" \
    "v_exp_f32 v77, v77\n\t" \
    "v_exp_f32 v78, v78\n\t" \
    "v_exp_f32 v79, v79\n\t" \
    "v_mfma_f32_32x32x16_bf16 %[ob], %[v1], v[108:111], %[ob]\n\t" \
    "ds_read_b128 %[v1], %[av] offset:704\n\t" \
    "v_cvt_pk_bf16_f32 v102, v76, v77\n\t" \
    "v_cvt_pk_bf16_f32 v103, v78, v79\n\t" \
    "v_exp_f32 v80, v80\n\t" \
    "v_exp_f32 v81, v81\n\t" \
    "v_exp_f32 v82, v82\n\t" \
    "v_exp_f32 v83, v83\n\t" \
    "s_waitcnt lgkmcnt(1)\n\t" \
    "v_mfma_f32_32x32x16_bf16 v[64:79], %[ka], %[qaa], 0\n\t" \
    "ds_read_b32 %[vpoll], %[ard] offset:0\n\t" \
    "v_cvt_pk_bf16_f32 v104, v80, v81\n\t" \
    "v_cvt_pk_bf16_f32 v105, v82, v83\n\t" \
    "v_exp_f32 v84, v84\n\t" \
    "v_exp_f32 v85, v85\n\t" \
    "v_exp_f32 v86, v86\n\t" \
    "v_exp_f32 v87, v87\n\t" \
    "v_mfma_f32_32x32x8bf16_1k v[64:79], %[kb], %[qba], v[64:79]\n\t" \
    "v_cvt_pk_bf16_f32 v106, v84, v85\n\t" \
    "v_cvt_pk_bf16_f32 v107, v86, v87\n\t" \
    "v_exp_f32 v88, v88\n\t" \
    "v_exp_f32 v89, v89\n\t" \
    "v_exp_f32 v90, v90\n\t" \
    "v_exp_f32 v91, v91\n\t" \
    "s_waitcnt lgkmcnt(0)\n\t" \
    "v_mfma_f32_32x32x16_bf16 %[oa], %[v1], v[100:103], %[oa]\n\t" \
    "v_cvt_pk_bf16_f32 v108, v88, v89\n\t" \
    "v_cvt_pk_bf16_f32 v109, v90, v91\n\t" \
    "v_exp_f32 v92, v92\n\t" \
    "v_exp_f32 v93, v93\n\t" \
    "v_exp_f32 v94, v94\n\t" \
    "v_exp_f32 v95, v95\n\t" \
    "v_cmp_lt_u32 vcc, %[vpoll], %[vit]\n\t" \
    "s_cbranch_vccz 10f\n\t" \
    "s_mov_b32 %[sspin], 0\n\t" \
    "11:\n\t" \
    "s_sleep 1\n\t" \
    "ds_read_b32 %[vpoll], %[ard] offset:0\n\t" \
    "s_add_u32 %[sspin], %[sspin], 1\n\t" \
    "s_waitcnt lgkmcnt(0)\n\t" \
    "s_cmp_gt_u32 %[sspin], 0x40000\n\t" \
    "s_cbranch_scc1 12f\n\t" \
    "v_cmp_lt_u32 vcc, %[vpoll], %[vit]\n\t" \
    "s_cbranch_vccnz 11b\n\t" \
    "12:\n\t" \
    "s_cmp_eq_u32 %[sc], %[slast]\n\t" \
    "10:\n\t" \
    "v_mfma_f32_32x32x16_bf16 %[ob], %[v0], v[104:107], %[ob]\n\t" \
    "ds_read_b128 %[v0], %[av] offset:3072\n\t" \
    "v_cvt_pk_bf16_f32 v110, v92, v93\n\t" \
    "v_cvt_pk_bf16_f32 v111, v94, v95\n\t" \
    "v_exp_f32 v64, v64\n\t" \
    "v_exp_f32 v65, v65\n\t" \
    "v_exp_f32 v66, v66\n\t" \
    "v_exp_f32 v67, v67\n\t" \
    "v_mfma_f32_32x32x16_bf16 v[80:95], %[ka], %[qab], 0\n\t" \
    "ds_read_b128 %[ka], %[ak] offset:6144\n\t" \
    "v_cvt_pk_bf16_f32 v96, v64, v65\n\t" \
    "v_cvt_pk_bf16_f32 v97, v66, v67\n\t" \
    "v_exp_f32 v68, v68\n\t" \
    "v_exp_f32 v69, v69\n\t" \
    "v_exp_f32 v70, v70\n\t" \
    "v_exp_f32 v71, v71\n\t" \
    "v_mfma_f32_32x32x8bf16_1k v[80:95], %[kb], %[qbb], v[80:95]\n\t" \
    "ds_read_b64 %[kb], %[akb] offset:6144\n\t" \
    "v_cvt_pk_bf16_f32 v98, v68, v69\n\t" \
    "v_cvt_pk_bf16_f32 v99, v70, v71\n\t" \
    "v_exp_f32 v72, v72\n\t" \
    "v_exp_f32 v73, v73\n\t" \
    "v_exp_f32 v74, v74\n\t" \
    "v_exp_f32 v75, v75\n\t" \
    "s_waitcnt lgkmcnt(2)\n\t" \
    "v_mfma_f32_32x32x16_bf16 %[oa], %[v0], v[96:99], %[oa]\n\t" \
    "v_cvt_pk_bf16_f32 v100, v72, v73\n\t" \
    "v_cvt_pk_bf16_f32 v101, v74, v75\n\t" \
    "v_exp_f32 v76, v76\n\t" \
    "v_exp_f32 v77, v77\n\t" \
    "v_exp_f32 v78, v78\n\t" \
    "v_exp_f32 v79, v79\n\t" \
    "v_mfma_f32_32x32x16_bf16 %[ob], %[v1], v[108:111], %[ob]\n\t" \
    "ds_read_b128 %[v1], %[av] offset:3776\n\t" \
    "v_cvt_pk_bf16_f32 v102, v76, v77\n\t" \
    "v_cvt_pk_bf16_f32 v103, v78, v79\n\t" \
    "v_exp_f32 v80, v80\n\t" \
    "v_exp_f32 v81, v81\n\t" \
    "v_exp_f32 v82, v82\n\t" \
    "v_exp_f32 v83, v83\n\t" \
    "s_waitcnt lgkmcnt(1)\n\t" \
    "v_mfma_f32_32x32x16_bf16 v[64:79], %[ka], %[qaa], 0\n\t" \
    "v_cvt_pk_bf16_f32 v104, v80, v81\n\t" \
    "v_cvt_pk_bf16_f32 v105, v82, v83\n\t" \
    "v_exp_f32 v84, v84\n\t" \
    "v_exp_f32 v85, v85\n\t" \
    "v_exp_f32 v86, v86\n\t" \
    "v_exp_f32 v87, v87\n\t" \
    "v_mfma_f32_32x32x8bf16_1k v[64:79], %[kb], %[qba], v[64:79]\n\t" \
    "v_cvt_pk_bf16_f32 v106, v84, v85\n\t" \
    "v_cvt_pk_bf16_f32 v107, v86, v87\n\t" \
    "v_exp_f32 v88, v88\n\t" \
    "v_exp_f32 v89, v89\n\t" \
    "v_exp_f32 v90, v90\n\t" \
    "v_exp_f32 v91, v91\n\t" \
    "s_waitcnt lgkmcnt(0)\n\t" \
    "v_mfma_f32_32x32x16_bf16 %[oa], %[v1], v[100:103], %[oa]\n\t" \
    "v_cvt_pk_bf16_f32 v108, v88, v89\n\t" \
    "v_cvt_pk_bf16_f32 v109, v90, v91\n\t" \
    "v_exp_f32 v92, v92\n\t" \
    "v_exp_f32 v93, v93\n\t" \
    "v_exp_f32 v94, v94\n\t" \
    "v_exp_f32 v95, v95\n\t" \
    "s_waitcnt vmcnt(0)\n\t" \
    "ds_write_b32 %[awr], %[vit] offset:16\n\t" \
    "v_mfma_f32_32x32x16_bf16 %[ob], %[v0], v[104:107], %[ob]\n\t" \
    "ds_read_b128 %[v0], %[av] offset:6144\n\t" \
    "v_cvt_pk_bf16_f32 v110, v92, v93\n\t" \
    "v_cvt_pk_bf16_f32 v111, v94, v95\n\t" \
    "v_exp_f32 v64, v64\n\t" \
    "v_exp_f32 v65, v65\n\t" \
    "v_exp_f32 v66, v66\n\t" \
    "v_exp_f32 v67, v67\n\t" \
    "v_mfma_f32_32x32x16_bf16 v[80:95], %[ka], %[qab], 0\n\t" \
    "ds_read_b128 %[ka], %[ak] offset:9216\n\t" \
    "v_cvt_pk_bf16_f32 v96, v64, v65\n\t" \
    "v_cvt_pk_bf16_f32 v97, v66, v67\n\t" \
    "v_exp_f32 v68, v68\n\t" \
    "v_exp_f32 v69, v69\n\t" \
    "v_exp_f32 v70, v70\n\t" \
    "v_exp_f32 v71, v71\n\t" \
    "v_mfma_f32_32x32x8bf16_1k v[80:95], %[kb], %[qbb], v[80:95]\n\t" \
    "ds_read_b64 %[kb], %[akb] offset:9216\n\t" \
    "v_cvt_pk_bf16_f32 v98, v68, v69\n\t" \
    "v_cvt_pk_bf16_f32 v99, v70, v71\n\t" \
    "v_exp_f32 v72, v72\n\t" \
    "v_exp_f32 v73, v73\n\t" \
    "v_exp_f32 v74, v74\n\t" \
    "v_exp_f32 v75, v75\n\t" \
    "s_waitcnt lgkmcnt(2)\n\t" \
    "v_mfma_f32_32x32x16_bf16 %[oa], %[v0], v[96:99], %[oa]\n\t" \
    "v_cvt_pk_bf16_f32 v100, v72, v73\n\t" \
    "v_cvt_pk_bf16_f32 v101, v74, v75\n\t" \
    "v_exp_f32 v76, v76\n\t" \
    "v_exp_f32 v77, v77\n\t" \
    "v_exp_f32 v78, v78\n\t" \
    "v_exp_f32 v79, v79\n\t" \
    "v_mfma_f32_32x32x16_bf16 %[ob], %[v1], v[108:111], %[ob]\n\t" \
    "ds_read_b128 %[v1], %[av] offset:6848\n\t" \
    "v_cvt_pk_bf16_f32 v102, v76, v77\n\t" \
    "v_cvt_pk_bf16_f32 v103, v78, v79\n\t" \
    "v_exp_f32 v80, v80\n\t" \
    "v_exp_f32 v81, v81\n\t" \
    "v_exp_f32 v82, v82\n\t" \
    "v_exp_f32 v83, v83\n\t" \
    "s_waitcnt lgkmcnt(1)\n\t" \
    "v_mfma_f32_32x32x16_bf16 v[64:79], %[ka], %[qaa], 0\n\t" \
    "ds_read_b32 %[vpoll], %[ard] offset:16\n\t" \
    "v_cvt_pk_bf16_f32 v104, v80, v81\n\t" \
    "v_cvt_pk_bf16_f32 v105, v82, v83\n\t" \
    "v_exp_f32 v84, v84\n\t" \
    "v_exp_f32 v85, v85\n\t" \
    "v_exp_f32 v86, v86\n\t" \
    "v_exp_f32 v87, v87\n\t" \
    "v_mfma_f32_32x32x8bf16_1k v[64:79], %[kb], %[qba], v[64:79]\n\t" \
    "v_cvt_pk_bf16_f32 v106, v84, v85\n\t" \
    "v_cvt_pk_bf16_f32 v107, v86, v87\n\t" \
    "v_exp_f32 v88, v88\n\t" \
    "v_exp_f32 v89, v89\n\t" \
    "v_exp_f32 v90, v90\n\t" \
    "v_exp_f32 v91, v91\n\t" \
    "s_waitcnt lgkmcnt(0)\n\t" \
    "v_mfma_f32_32x32x16_bf16 %[oa], %[v1], v[100:103], %[oa]\n\t" \
    "v_cvt_pk_bf16_f32 v108, v88, v89\n\t" \
    "v_cvt_pk_bf16_f32 v109, v90, v91\n\t" \
    "v_exp_f32 v92, v92\n\t" \
    "v_exp_f32 v93, v93\n\t" \
    "v_exp_f32 v94, v94\n\t" \
    "v_exp_f32 v95, v95\n\t" \
    "v_cmp_lt_u32 vcc, %[vpoll], %[vit]\n\t" \
    "s_cbranch_vccz 13f\n\t" \
    "s_mov_b32 %[sspin], 0\n\t" \
    "14:\n\t" \
    "s_sleep 1\n\t" \
    "ds_read_b32 %[vpoll], %[ard] offset:16\n\t" \
    "s_add_u32 %[sspin], %[sspin], 1\n\t" \
    "s_waitcnt lgkmcnt(0)\n\t" \
    "s_cmp_gt_u32 %[sspin], 0x40000\n\t" \
    "s_cbranch_scc1 15f\n\t" \
    "v_cmp_lt_u32 vcc, %[vpoll], %[vit]\n\t" \
    "s_cbranch_vccnz 14b\n\t" \
    "15:\n\t" \
    "s_cmp_eq_u32 %[sc], %[slast]\n\t" \
    "13:\n\t" \
    "s_mov_b32 m0, %[sd0]\n\t" \
    "s_cbranch_scc1 16f\n\t" \
    "global_load_lds_dwordx4 %[voff], %[sg] sc1\n\t" \
    "global_load_lds_dwordx4 %[voff], %[sg] offset:1024 sc1\n\t" \
    "global_load_lds_dwordx4 %[voff], %[sg] offset:2048 sc1\n\t" \
    "v_add_u32 %[voff], 12288, %[voff]\n\t" \
    "16:\n\t" \
    "v_mfma_f32_32x32x16_bf16 %[ob], %[v0], v[104:107], %[ob]\n\t" \
    "ds_read_b128 %[v0], %[av] offset:9216\n\t" \
    "v_cvt_pk_bf16_f32 v110, v92, v93\n\t" \
    "v_cvt_pk_bf16_f32 v111, v94, v95\n\t" \
    "v_exp_f32 v64, v64\n\t" \
    "v_exp_f32 v65, v65\n\t" \
    "v_exp_f32 v66, v66\n\t" \
    "v_exp_f32 v67, v67\n\t" \
    "v_mfma_f32_32x32x16_bf16 v[80:95], %[ka], %[qab], 0\n\t" \
    "ds_read_b128 %[ka], %[ak] offset:24576\n\t" \
    "v_cvt_pk_bf16_f32 v96, v64, v65\n\t" \
    "v_cvt_pk_bf16_f32 v97, v66, v67\n\t" \
    "v_exp_f32 v68, v68\n\t" \
    "v_exp_f32 v69, v69\n\t" \
    "v_exp_f32 v70, v70\n\t" \
    "v_exp_f32 v71, v71\n\t" \
    "v_mfma_f32_32x32x8bf16_1k v[80:95], %[kb], %[qbb], v[80:95]\n\t" \
    "ds_read_b64 %[kb], %[akb] offset:24576\n\t" \
    "v_cvt_pk_bf16_f32 v98, v68, v69\n\t" \
    "v_cvt_pk_bf16_f32 v99, v70, v71\n\t" \
    "v_exp_f32 v72, v72\n\t" \
    "v_exp_f32 v73, v73\n\t" \
    "v_exp_f32 v74, v74\n\t" \
    "v_exp_f32 v75, v75\n\t" \
    "s_waitcnt lgkmcnt(2)\n\t" \
    "v_mfma_f32_32x32x16_bf16 %[oa], %[v0], v[96:99], %[oa]\n\t" \
    "v_cvt_pk_bf16_f32 v100, v72, v73\n\t" \
    "v_cvt_pk_bf16_f32 v101, v74, v75\n\t" \
    "v_exp_f32 v76, v76\n\t" \
    "v_exp_f32 v77, v77\n\t" \
    "v_exp_f32 v78, v78\n\t" \
    "v_exp_f32 v79, v79\n\t" \
    "v_mfma_f32_32x32x16_bf16 %[ob], %[v1], v[108:111], %[ob]\n\t" \
    "ds_read_b128 %[v1], %[av] offset:9920\n\t" \
    "v_cvt_pk_bf16_f32 v102, v76, v77\n\t" \
    "v_cvt_pk_bf16_f32 v103, v78, v79\n\t" \
    "v_exp_f32 v80, v80\n\t" \
    "v_exp_f32 v81, v81\n\t" \
    "v_exp_f32 v82, v82\n\t" \
    "v_exp_f32 v83, v83\n\t" \
    "s_waitcnt lgkmcnt(1)\n\t" \
    "v_mfma_f32_32x32x16_bf16 v[64:79], %[ka], %[qaa], 0\n\t" \
    "v_cvt_pk_bf16_f32 v104, v80, v81\n\t" \
    "v_cvt_pk_bf16_f32 v105, v82, v83\n\t" \
    "v_exp_f32 v84, v84\n\t" \
    "v_exp_f32 v85, v85\n\t" \
    "v_exp_f32 v86, v86\n\t" \
    "v_exp_f32 v87, v87\n\t" \
    "v_mfma_f32_32x32x8bf16_1k v[64:79], %[kb], %[qba], v[64:79]\n\t" \
    "v_cvt_pk_bf16_f32 v106, v84, v85\n\t" \
    "v_cvt_pk_bf16_f32 v107, v86, v87\n\t" \
    "v_exp_f32 v88, v88\n\t" \
    "v_exp_f32 v89, v89\n\t" \
    "v_exp_f32 v90, v90\n\t" \
    "v_exp_f32 v91, v91\n\t" \
    "s_waitcnt lgkmcnt(0)\n\t" \
    "v_mfma_f32_32x32x16_bf16 %[oa], %[v1], v[100:103], %[oa]\n\t" \
    "v_cvt_pk_bf16_f32 v108, v88, v89\n\t" \
    "v_cvt_pk_bf16_f32 v109, v90, v91\n\t" \
    "v_exp_f32 v92, v92\n\t" \
    "v_exp_f32 v93, v93\n\t" \
    "v_exp_f32 v94, v94\n\t" \
    "v_exp_f32 v95, v95\n\t" \
    "ds_write_b32 %[awr], %[vit] offset:32\n\t" \
    "v_mfma_f32_32x32x16_bf16 %[ob], %[v0], v[104:107], %[ob]\n\t" \
    "ds_read_b128 %[v0], %[av] offset:24576\n\t" \
    "v_cvt_pk_bf16_f32 v110, v92, v93\n\t" \
    "v_cvt_pk_bf16_f32 v111, v94, v95\n\t" \
    "v_exp_f32 v64, v64\n\t" \
    "v_exp_f32 v65, v65\n\t" \
    "v_exp_f32 v66, v66\n\t" \
    "v_exp_f32 v67, v67\n\t" \
    "v_mfma_f32_32x32x16_bf16 v[80:95], %[ka], %[qab], 0\n\t" \
    "ds_read_b128 %[ka], %[ak] offset:27648\n\t" \
    "v_cvt_pk_bf16_f32 v96, v64, v65\n\t" \
    "v_cvt_pk_bf16_f32 v97, v66, v67\n\t" \
    "v_exp_f32 v68, v68\n\t" \
    "v_exp_f32 v69, v69\n\t" \
    "v_exp_f32 v70, v70\n\t" \
    "v_exp_f32 v71, v71\n\t" \
    "v_mfma_f32_32x32x8bf16_1k v[80:95], %[kb], %[qbb], v[80:95]\n\t" \
    "ds_read_b64 %[kb], %[akb] offset:27648\n\t" \
    "v_cvt_pk_bf16_f32 v98, v68, v69\n\t" \
    "v_cvt_pk_bf16_f32 v99, v70, v71\n\t" \
    "v_exp_f32 v72, v72\n\t" \
    "v_exp_f32 v73, v73\n\t" \
    "v_exp_f32 v74, v74\n\t" \
    "v_exp_f32 v75, v75\n\t" \
    "s_waitcnt lgkmcnt(2)\n\t" \
    "v_mfma_f32_32x32x16_bf16 %[oa], %[v0], v[96:99], %[oa]\n\t" \
    "v_cvt_pk_bf16_f32 v100, v72, v73\n\t" \
    "v_cvt_pk_bf16_f32 v101, v74, v75\n\t" \
    "v_exp_f32 v76, v76\n\t" \
    "v_exp_f32 v77, v77\n\t" \
    "v_exp_f32 v78, v78\n\t" \
    "v_exp_f32 v79, v79\n\t" \
    "v_mfma_f32_32x32x16_bf16 %[ob], %[v1], v[108:111], %[ob]\n\t" \
    "ds_read_b128 %[v1], %[av] offset:25280\n\t" \
    "v_cvt_pk_bf16_f32 v102, v76, v77\n\t" \
    "v_cvt_pk_bf16_f32 v103, v78, v79\n\t" \
    "v_exp_f32 v80, v80\n\t" \
    "v_exp_f32 v81, v81\n\t" \
    "v_exp_f32 v82, v82\n\t" \
    "v_exp_f32 v83, v83\n\t" \
    "s_waitcnt lgkmcnt(1)\n\t" \
    "v_mfma_f32_32x32x16_bf16 v[64:79], %[ka], %[qaa], 0\n\t" \
    "ds_read_b32 %[vpoll], %[ard] offset:32\n\t" \
    "v_cvt_pk_bf16_f32 v104, v80, v81\n\t" \
    "v_cvt_pk_bf16_f32 v105, v82, v83\n\t" \
    "v_exp_f32 v84, v84\n\t" \
    "v_exp_f32 v85, v85\n\t" \
    "v_exp_f32 v86, v86\n\t" \
    "v_exp_f32 v87, v87\n\t" \
    "v_mfma_f32_32x32x8bf16_1k v[64:79], %[kb], %[qba], v[64:79]\n\t" \
    "v_cvt_pk_bf16_f32 v106, v84, v85\n\t" \
    "v_cvt_pk_bf16_f32 v107, v86, v87\n\t" \
    "v_exp_f32 v88, v88\n\t" \
    "v_exp_f32 v89, v89\n\t" \
    "v_exp_f32 v90, v90\n\t" \
    "v_exp_f32 v91, v91\n\t" \
    "s_waitcnt lgkmcnt(0)\n\t" \
    "v_mfma_f32_32x32x16_bf16 %[oa], %[v1], v[100:103], %[oa]\n\t" \
    "v_cvt_pk_bf16_f32 v108, v88, v89\n\t" \
    "v_cvt_pk_bf16_f32 v109, v90, v91\n\t" \
    "v_exp_f32 v92, v92\n\t" \
    "v_exp_f32 v93, v93\n\t" \
    "v_exp_f32 v94, v94\n\t" \
    "v_exp_f32 v95, v95\n\t" \
    "v_cmp_lt_u32 vcc, %[vpoll], %[vit]\n\t" \
    "s_cbranch_vccz 17f\n\t" \
    "s_mov_b32 %[sspin], 0\n\t" \
    "18:\n\t" \
    "s_sleep 1\n\t" \
    "ds_read_b32 %[vpoll], %[ard] offset:32\n\t" \
    "s_add_u32 %[sspin], %[sspin], 1\n\t" \
    "s_waitcnt lgkmcnt(0)\n\t" \
    "s_cmp_gt_u32 %[sspin], 0x40000\n\t" \
    "s_cbranch_scc1 19f\n\t" \
    "v_cmp_lt_u32 vcc, %[vpoll], %[vit]\n\t" \
    "s_cbranch_vccnz 18b\n\t" \
    "19:\n\t" \
    "s_cmp_eq_u32 %[sc], %[slast]\n\t" \
    "17:\n\t" \
    "v_mfma_f32_32x32x16_bf16 %[ob], %[v0], v[104:107], %[ob]\n\t" \
    "ds_read_b128 %[v0], %[av] offset:27648\n\t" \
    "v_cvt_pk_bf16_f32 v110, v92, v93\n\t" \
    "v_cvt_pk_bf16_f32 v111, v94, v95\n\t" \
    "v_exp_f32 v64, v64\n\t" \
    "v_exp_f32 v65, v65\n\t" \
    "v_exp_f32 v66, v66\n\t" \
    "v_exp_f32 v67, v67\n\t" \
    "v_mfma_f32_32x32x16_bf16 v[80:95], %[ka], %[qab], 0\n\t" \
    "ds_read_b128 %[ka], %[ak] offset:30720\n\t" \
    "v_cvt_pk_bf16_f32 v96, v64, v65\n\t" \
    "v_cvt_pk_bf16_f32 v97, v66, v67\n\t" \
    "v_exp_f32 v68, v68\n\t" \
    "v_exp_f32 v69, v69\n\t" \
    "v_exp_f32 v70, v70\n\t" \
    "v_exp_f32 v71, v71\n\t" \
    "v_mfma_f32_32x32x8bf16_1k v[80:95], %[kb], %[qbb], v[80:95]\n\t" \
    "ds_read_b64 %[kb], %[akb] offset:30720\n\t" \
    "v_cvt_pk_bf16_f32 v98, v68, v69\n\t" \
    "v_cvt_pk_bf16_f32 v99, v70, v71\n\t" \
    "v_exp_f32 v72, v72\n\t" \
    "v_exp_f32 v73, v73\n\t" \
    "v_exp_f32 v74, v74\n\t" \
    "v_exp_f32 v75, v75\n\t" \
    "s_waitcnt lgkmcnt(2)\n\t" \
    "v_mfma_f32_32x32x16_bf16 %[oa], %[v0], v[96:99], %[oa]\n\t" \
    "v_cvt_pk_bf16_f32 v100, v72, v73\n\t" \
    "v_cvt_pk_bf16_f32 v101, v74, v75\n\t" \
    "v_exp_f32 v76, v76\n\t" \
    "v_exp_f32 v77, v77\n\t" \
    "v_exp_f32 v78, v78\n\t" \
    "v_exp_f32 v79, v79\n\t" \
    "v_mfma_f32_32x32x16_bf16 %[ob], %[v1], v[108:111], %[ob]\n\t" \
    "ds_read_b128 %[v1], %[av] offset:28352\n\t" \
    "v_cvt_pk_bf16_f32 v102, v76, v77\n\t" \
    "v_cvt_pk_bf16_f32 v103, v78, v79\n\t" \
    "v_exp_f32 v80, v80\n\t" \
    "v_exp_f32 v81, v81\n\t" \
    "v_exp_f32 v82, v82\n\t" \
    "v_exp_f32 v83, v83\n\t" \
    "s_waitcnt lgkmcnt(1)\n\t" \
    "v_mfma_f32_32x32x16_bf16 v[64:79], %[ka], %[qaa], 0\n\t" \
    "v_cvt_pk_bf16_f32 v104, v80, v81\n\t" \
    "v_cvt_pk_bf16_f32 v105, v82, v83\n\t" \
    "v_exp_f32 v84, v84\n\t" \
    "v_exp_f32 v85, v85\n\t" \
    "v_exp_f32 v86, v86\n\t" \
    "v_exp_f32 v87, v87\n\t" \
    "v_mfma_f32_32x32x8bf16_1k v[64:79], %[kb], %[qba], v[64:79]\n\t" \
    "v_cvt_pk_bf16_f32 v106, v84, v85\n\t" \
    "v_cvt_pk_bf16_f32 v107, v86, v87\n\t" \
    "v_exp_f32 v88, v88\n\t" \
    "v_exp_f32 v89, v89\n\t" \
    "v_exp_f32 v90, v90\n\t" \
    "v_exp_f32 v91, v91\n\t" \
    "s_waitcnt lgkmcnt(0)\n\t" \
    "v_mfma_f32_32x32x16_bf16 %[oa], %[v1], v[100:103], %[oa]\n\t" \
    "v_cvt_pk_bf16_f32 v108, v88, v89\n\t" \
    "v_cvt_pk_bf16_f32 v109, v90, v91\n\t" \
    "v_exp_f32 v92, v92\n\t" \
    "v_exp_f32 v93, v93\n\t" \
    "v_exp_f32 v94, v94\n\t" \
    "v_exp_f32 v95, v95\n\t" \
    "s_waitcnt vmcnt(0)\n\t" \
    "ds_write_b32 %[awr], %[vit] offset:48\n\t" \
    "v_mfma_f32_32x32x16_bf16 %[ob], %[v0], v[104:107], %[ob]\n\t" \
    "ds_read_b128 %[v0], %[av] offset:30720\n\t" \
    "v_cvt_pk_bf16_f32 v110, v92, v93\n\t" \
    "v_cvt_pk_bf16_f32 v111, v94, v95\n\t" \
    "v_exp_f32 v64, v64\n\t" \
    "v_exp_f32 v65, v65\n\t" \
    "v_exp_f32 v66, v66\n\t" \
    "v_exp_f32 v67, v67\n\t" \
    "v_mfma_f32_32x32x16_bf16 v[80:95], %[ka], %[qab], 0\n\t" \
    "ds_read_b128 %[ka], %[ak] offset:33792\n\t" \
    "v_cvt_pk_bf16_f32 v96, v64, v65\n\t" \
    "v_cvt_pk_bf16_f32 v97, v66, v67\n\t" \
    "v_exp_f32 v68, v68\n\t" \
    "v_exp_f32 v69, v69\n\t" \
    "v_exp_f32 v70, v70\n\t" \
    "v_exp_f32 v71, v71\n\t" \
    "v_mfma_f32_32x32x8bf16_1k v[80:95], %[kb], %[qbb], v[80:95]\n\t" \
    "ds_read_b64 %[kb], %[akb] offset:33792\n\t" \
    "v_cvt_pk_bf16_f32 v98, v68, v69\n\t" \
    "v_cvt_pk_bf16_f32 v99, v70, v71\n\t" \
    "v_exp_f32 v72, v72\n\t" \
    "v_exp_f32 v73, v73\n\t" \
    "v_exp_f32 v74, v74\n\t" \
    "v_exp_f32 v75, v75\n\t" \
    "s_waitcnt lgkmcnt(2)\n\t" \
    "v_mfma_f32_32x32x16_bf16 %[oa], %[v0], v[96:99], %[oa]\n\t" \
    "v_cvt_pk_bf16_f32 v100, v72, v73\n\t" \
    "v_cvt_pk_bf16_f32 v101, v74, v75\n\t" \
    "v_exp_f32 v76, v76\n\t" \
    "v_exp_f32 v77, v77\n\t" \
    "v_exp_f32 v78, v78\n\t" \
    "v_exp_f32 v79, v79\n\t" \
    "v_mfma_f32_32x32x16_bf16 %[ob], %[v1], v[108:111], %[ob]\n\t" \
    "ds_read_b128 %[v1], %[av] offset:31424\n\t" \
    "v_cvt_pk_bf16_f32 v102, v76, v77\n\t" \
    "v_cvt_pk_bf16_f32 v103, v78, v79\n\t" \
    "v_exp_f32 v80, v80\n\t" \
    "v_exp_f32 v81, v81\n\t" \
    "v_exp_f32 v82, v82\n\t" \
    "v_exp_f32 v83, v83\n\t" \
    "s_waitcnt lgkmcnt(1)\n\t" \
    "v_mfma_f32_32x32x16_bf16 v[64:79], %[ka], %[qaa], 0\n\t" \
    "ds_read_b32 %[vpoll], %[ard] offset:48\n\t" \
    "v_cvt_pk_bf16_f32 v104, v80, v81\n\t" \
    "v_cvt_pk_bf16_f32 v105, v82, v83\n\t" \
    "v_exp_f32 v84, v84\n\t" \
    "v_exp_f32 v85, v85\n\t" \
    "v_exp_f32 v86, v86\n\t" \
    "v_exp_f32 v87, v87\n\t" \
    "v_mfma_f32_32x32x8bf16_1k v[64:79], %[kb], %[qba], v[64:79]\n\t" \
    "v_cvt_pk_bf16_f32 v106, v84, v85\n\t" \
    "v_cvt_pk_bf16_f32 v107, v86, v87\n\t" \
    "v_exp_f32 v88, v88\n\t" \
    "v_exp_f32 v89, v89\n\t" \
    "v_exp_f32 v90, v90\n\t" \
    "v_exp_f32 v91, v91\n\t" \
    "s_waitcnt lgkmcnt(0)\n\t" \
    "v_mfma_f32_32x32x16_bf16 %[oa], %[v1], v[100:103], %[oa]\n\t" \
    "v_cvt_pk_bf16_f32 v108, v88, v89\n\t" \
    "v_cvt_pk_bf16_f32 v109, v90, v91\n\t" \
    "v_exp_f32 v92, v92\n\t" \
    "v_exp_f32 v93, v93\n\t" \
    "v_exp_f32 v94, v94\n\t" \
    "v_exp_f32 v95, v95\n\t" \
    "v_cmp_lt_u32 vcc, %[vpoll], %[vit]\n\t" \
    "s_cbranch_vccz 20f\n\t" \
    "s_mov_b32 %[sspin], 0\n\t" \
    "21:\n\t" \
    "s_sleep 1\n\t" \
    "ds_read_b32 %[vpoll], %[ard] offset:48\n\t" \
    "s_add_u32 %[sspin], %[sspin], 1\n\t" \
    "s_waitcnt lgkmcnt(0)\n\t" \
    "s_cmp_gt_u32 %[sspin], 0x40000\n\t" \
    "s_cbranch_scc1 22f\n\t" \
    "v_cmp_lt_u32 vcc, %[vpoll], %[vit]\n\t" \
    "s_cbranch_vccnz 21b\n\t" \
    "22:\n\t" \
    "s_cmp_eq_u32 %[sc], %[slast]\n\t" \
    "20:\n\t" \
    "s_mov_b32 m0, %[sd2]\n\t" \
    "s_cbranch_scc1 23f\n\t" \
    "global_load_lds_dwordx4 %[voff], %[sg] sc1\n\t" \
    "global_load_lds_dwordx4 %[voff], %[sg] offset:1024 sc1\n\t" \
    "global_load_lds_dwordx4 %[voff], %[sg] offset:2048 sc1\n\t" \
    "v_add_u32 %[voff], 12288, %[voff]\n\t" \
    "23:\n\t" \
    "v_mfma_f32_32x32x16_bf16 %[ob], %[v0], v[104:107], %[ob]\n\t" \
    "ds_read_b128 %[v0], %[av] offset:33792\n\t" \
    "v_cvt_pk_bf16_f32 v110, v92, v93\n\t" \
    "v_cvt_pk_bf16_f32 v111, v94, v95\n\t" \
    "v_exp_f32 v64, v64\n\t" \
    "v_exp_f32 v65, v65\n\t" \
    "v_exp_f32 v66, v66\n\t" \
    "v_exp_f32 v67, v67\n\t" \
    "v_mfma_f32_32x32x16_bf16 v[80:95], %[ka], %[qab], 0\n\t" \
    "ds_read_b128 %[ka], %[ak] offset:0\n\t" \
    "v_cvt_pk_bf16_f32 v96, v64, v65\n\t" \
    "v_cvt_pk_bf16_f32 v97, v66, v67\n\t" \
    "v_exp_f32 v68, v68\n\t" \
    "v_exp_f32 v69, v69\n\t" \
    "v_exp_f32 v70, v70\n\t" \
    "v_exp_f32 v71, v71\n\t" \
    "v_mfma_f32_32x32x8bf16_1k v[80:95], %[kb], %[qbb], v[80:95]\n\t" \
    "ds_read_b64 %[kb], %[akb] offset:0\n\t" \
    "v_cvt_pk_bf16_f32 v98, v68, v69\n\t" \
    "v_cvt_pk_bf16_f32 v99, v70, v71\n\t" \
    "v_exp_f32 v72, v72\n\t" \
    "v_exp_f32 v73, v73\n\t" \
    "v_exp_f32 v74, v74\n\t" \
    "v_exp_f32 v75, v75\n\t" \
    "s_waitcnt lgkmcnt(2)\n\t" \
    "v_mfma_f32_32x32x16_bf16 %[oa], %[v0], v[96:99], %[oa]\n\t" \
    "v_cvt_pk_bf16_f32 v100, v72, v73\n\t" \
    "v_cvt_pk_bf16_f32 v101, v74, v75\n\t" \
    "v_exp_f32 v76, v76\n\t" \
    "v_exp_f32 v77, v77\n\t" \
    "v_exp_f32 v78, v78\n\t" \
    "v_exp_f32 v79, v79\n\t" \
    "v_mfma_f32_32x32x16_bf16 %[ob], %[v1], v[108:111], %[ob]\n\t" \
    "ds_read_b128 %[v1], %[av] offset:34496\n\t" \
    "v_cvt_pk_bf16_f32 v102, v76, v77\n\t" \
    "v_cvt_pk_bf16_f32 v103, v78, v79\n\t" \
    "v_exp_f32 v80, v80\n\t" \
    "v_exp_f32 v81, v81\n\t" \
    "v_exp_f32 v82, v82\n\t" \
    "v_exp_f32 v83, v83\n\t" \
    "s_waitcnt lgkmcnt(1)\n\t" \
    "v_mfma_f32_32x32x16_bf16 v[64:79], %[ka], %[qaa], 0\n\t" \
    "v_cvt_pk_bf16_f32 v104, v80, v81\n\t" \
    "v_cvt_pk_bf16_f32 v105, v82, v83\n\t" \
    "v_exp_f32 v84, v84\n\t" \
    "v_exp_f32 v85, v85\n\t" \
    "v_exp_f32 v86, v86\n\t" \
    "v_exp_f32 v87, v87\n\t" \
    "v_mfma_f32_32x32x8bf16_1k v[64:79], %[kb], %[qba], v[64:79]\n\t" \
    "v_cvt_pk_bf16_f32 v106, v84, v85\n\t" \
    "v_cvt_pk_bf16_f32 v107, v86, v87\n\t" \
    "v_exp_f32 v88, v88\n\t" \
    "v_exp_f32 v89, v89\n\t" \
    "v_exp_f32 v90, v90\n\t" \
    "v_exp_f32 v91, v91\n\t" \
    "s_waitcnt lgkmcnt(0)\n\t" \
    "v_mfma_f32_32x32x16_bf16 %[oa], %[v1], v[100:103], %[oa]\n\t" \
    "v_cvt_pk_bf16_f32 v108, v88, v89\n\t" \
    "v_cvt_pk_bf16_f32 v109, v90, v91\n\t" \
    "v_exp_f32 v92, v92\n\t" \
    "v_exp_f32 v93, v93\n\t" \
    "v_exp_f32 v94, v94\n\t" \
    "v_exp_f32 v95, v95\n\t" \
    "v_add_u32 %[vit], 1, %[vit]\n\t" \
    "s_add_u32 %[sc], %[sc], 1\n\t" \
    "s_cmp_le_u32 %[sc], %[slast]\n\t" \
    "s_cbranch_scc1 1b\n\t" \
    "v_cvt_pk_bf16_f32 v110, v92, v93\n\t" \
    "v_cvt_pk_bf16_f32 v111, v94, v95\n\t" \
    "s_nop 1\n\t" \
    "v_mfma_f32_32x32x16_bf16 %[ob], %[v0], v[104:107], %[ob]\n\t" \
    "v_mfma_f32_32x32x16_bf16 %[ob], %[v1], v[108:111], %[ob]\n\t" \
    "s_mov_b32 m0, %[sm0]\n\t" \
    "s_nop 15\n\t"
#define ATTN_ASM_TEXT1 \
    "s_mov_b32 %[sm0], m0\n\t" \
    "ds_read_b128 %[ka], %[ak]\n\t" \
    "ds_read_b64 %[kb], %[akb]\n\t" \
    "ds_read_b128 %[v0], %[av]\n\t" \
    "ds_read_b128 %[v1], %[av] offset:704\n\t" \
    "v_mov_b32 v104, 0\n\t" \
    "v_mov_b32 v105, 0\n\t" \
    "v_mov_b32 v106, 0\n\t" \
    "v_mov_b32 v107, 0\n\t" \
    "v_mov_b32 v108, 0\n\t" \
    "v_mov_b32 v109, 0\n\t" \
    "v_mov_b32 v110, 0\n\t" \
    "v_mov_b32 v111, 0\n\t" \
    "v_mov_b32 v92, 0\n\t" \
    "v_mov_b32 v93, 0\n\t" \
    "v_mov_b32 v94, 0\n\t" \
    "v_mov_b32 v95, 0\n\t" \
    "v_mov_b32 %[vit], 1\n\t" \
    "s_mov_b32 %[sc], 0\n\t" \
    "s_waitcnt lgkmcnt(0)\n\t" \
    "v_mfma_f32_32x32x16_bf16 v[64:79], %[ka], %[qaa], 0\n\t" \
    "s_nop 7\n\t" \
    "s_nop 7\n\t" \
    "v_mfma_f32_32x32x8bf16_1k v[64:79], %[kb], %[qba], v[64:79]\n\t" \
    "s_nop 7\n\t" \
    "s_nop 7\n\t" \
    "1:\n\t" \
    "s_cmp_eq_u32 %[sc], %[slast]\n\t" \
    "s_waitcnt vmcnt(0)\n\t" \
    "ds_write_b32 %[awr], %[vit] offset:0\n\t" \
    "v_mfma_f32_32x32x16_bf16 %[ob], %[v0], v[104:107], %[ob]\n\t" \
    "ds_read_b128 %[v0], %[av] offset:0\n\t" \
    "v_cvt_pk_bf16_f32 v110, v92, v93\n\t" \
    "v_cvt_pk_bf16_f32 v111, v94, v95\n\t" \
    "v_exp_f32 v64, v64\n\t" \
    "v_exp_f32 v65, v65\n\t" \
    "v_exp_f32 v66, v66\n\t" \
    "v_exp_f32 v67, v67\n\t" \
    "v_mfma_f32_32x32x16_bf16 v[80:95], %[ka], %[qab], 0\n\t" \
    "ds_read_b128 %[ka], %[ak] offset:3072\n\t" \
    "v_cvt_pk_bf16_f32 v96, v64, v65\n\t" \
    "v_cvt_pk_bf16_f32 v97, v66, v67\n\t" \
    "v_exp_f32 v68, v68\n\t" \
    "v_exp_f32 v69, v69\n\t" \
    "v_exp_f32 v70, v70\n\t" \
    "v_exp_f32 v71, v71\n\t" \
    "v_mfma_f32_32x32x8bf16_1k v[80:95], %[kb], %[qbb], v[80:95]\n\t" \
    "ds_read_b64 %[kb], %[akb] offset:3072\n\t" \
    "v_cvt_pk_bf16_f32 v98, v68, v69\n\t" \
    "v_cvt_pk_bf16_f32 v99, v70, v71\n\t" \
    "v_exp_f32 v72, v72\n\t" \
    "v_exp_f32 v73, v73\n\t" \
    "v_exp_f32 v74, v74\n\t" \
    "v_exp_f32 v75, v75\n\t" \
    "s_waitcnt lgkmcnt(2)\n\t" \
    "v_mfma_f32_32x32x16_bf16 %[oa], %[v0], v[96:99], %[oa]\n\t" \
    "v_cvt_pk_bf16_f32 v100, v72, v73\n\t" \
    "v_cvt_pk_bf16_f32 v101, v74, v75\n\t" \
    "v_exp_f32 v76, v76\n\t" \
    "v_exp_f32 v77, v77\n\t" \
    "v_exp_f32 v78, v78\n\t" \
    "v_exp_f32 v79, v79\n\t" \
    "v_mfma_f32_32x32x16_bf16 %[ob], %[v1], v[108:111], %[ob]\n\t" \
    "ds_read_b128 %[v1], %[av] offset:704\n\t" \
    "v_cvt_pk_bf16_f32 v102, v76, v77\n\t" \
    "v_cvt_pk_bf16_f32 v103, v78, v79\n\t" \
    "v_exp_f32 v80, v80\n\t" \
    "v_exp_f32 v81, v81\n\t" \
    "v_exp_f32 v82, v82\n\t" \
    "v_exp_f32 v83, v83\n\t" \
    "s_waitcnt lgkmcnt(1)\n\t" \
    "v_mfma_f32_32x32x16_bf16 v[64:79], %[ka], %[qaa], 0\n\t" \
    "ds_read_b32 %[vpoll], %[ard] offset:0\n\t" \
    "v_cvt_pk_bf16_f32 v104, v80, v81\n\t" \
    "v_cvt_pk_bf16_f32 v105, v82, v83\n\t" \
    "v_exp_f32 v84, v84\n\t" \
    "v_exp_f32 v85, v85\n\t" \
    "v_exp_f32 v86, v86\n\t" \
    "v_exp_f32 v87, v87\n\t" \
    "v_mfma_f32_32x32x8bf16_1k v[64:79], %[kb], %[qba], v[64:79]\n\t" \
    "v_cvt_pk_bf16_f32 v106, v84, v85\n\t" \
    "v_cvt_pk_bf16_f32 v107, v86, v87\n\t" \
    "v_exp_f32 v88, v88\n\t" \
    "v_exp_f32 v89, v89\n\t" \
    "v_exp_f32 v90, v90\n\t" \
    "v_exp_f32 v91, v91\n\t" \
    "s_waitcnt lgkmcnt(0)\n\t" \
    "v_mfma_f32_32x32x16_bf16 %[oa], %[v1], v[100:103], %[oa]\n\t" \
    "v_cvt_pk_bf16_f32 v108, v88, v89\n\t" \
    "v_cvt_pk_bf16_f32 v109, v90, v91\n\t" \
    "v_exp_f32 v92, v92\n\t" \
    "v_exp_f32 v93, v93\n\t" \
    "v_exp_f32 v94, v94\n\t" \
    "v_exp_f32 v95, v95\n\t" \
    "v_cmp_lt_u32 vcc, %[vpoll], %[vit]\n\t" \
    "s_cbranch_vccz 10f\n\t" \
    "s_mov_b32 %[sspin], 0\n\t" \
    "11:\n\t" \
    "s_sleep 1\n\t" \
    "ds_read_b32 %[vpoll], %[ard] offset:0\n\t" \
    "s_add_u32 %[sspin], %[sspin], 1\n\t" \
    "s_waitcnt lgkmcnt(0)\n\t" \
    "s_cmp_gt_u32 %[sspin], 0x40000\n\t" \
    "s_cbranch_scc1 12f\n\t" \
    "v_cmp_lt_u32 vcc, %[vpoll], %[vit]\n\t" \
    "s_cbranch_vccnz 11b\n\t" \
    "12:\n\t" \
    "s_cmp_eq_u32 %[sc], %[slast]\n\t" \
    "10:\n\t" \
    "s_mov_b32 m0, %[sd3]\n\t" \
    "s_nop 0\n\t" \
    "global_load_lds_dwordx4 %[voff], %[sg] sc1\n\t" \
    "global_load_lds_dwordx4 %[voff], %[sg] offset:1024 sc1\n\t" \
    "global_load_lds_dwordx4 %[voff], %[sg] offset:2048 sc1\n\t" \
    "v_add_u32 %[voff], 12288, %[voff]\n\t" \
    "v_mfma_f32_32x32x16_bf16 %[ob], %[v0], v[104:107], %[ob]\n\t" \
    "ds_read_b128 %[v0], %[av] offset:3072\n\t" \
    "v_cvt_pk_bf16_f32 v110, v92, v93\n\t" \
    "v_cvt_pk_bf16_f32 v111, v94, v95\n\t" \
    "v_exp_f32 v64, v64\n\t" \
    "v_exp_f32 v65, v65\n\t" \
    "v_exp_f32 v66, v66\n\t" \
    "v_exp_f32 v67, v67\n\t" \
    "v_mfma_f32_32x32x16_bf16 v[80:95], %[ka], %[qab], 0\n\t" \
    "ds_read_b128 %[ka], %[ak] offset:6144\n\t" \
    "v_cvt_pk_bf16_f32 v96, v64, v65\n\t" \
    "v_cvt_pk_bf16_f32 v97, v66, v67\n\t" \
    "v_exp_f32 v68, v68\n\t" \
    "v_exp_f32 v69, v69\n\t" \
    "v_exp_f32 v70, v70\n\t" \
    "v_exp_f32 v71, v71\n\t" \
    "v_mfma_f32_32x32x8bf16_1k v[80:95], %[kb], %[qbb], v[80:95]\n\t" \
    "ds_read_b64 %[kb], %[akb] offset:6144\n\t" \
    "v_cvt_pk_bf16_f32 v98, v68, v69\n\t" \
    "v_cvt_pk_bf16_f32 v99, v70, v71\n\t" \
    "v_exp_f32 v72, v72\n\t" \
    "v_exp_f32 v73, v73\n\t" \
    "v_exp_f32 v74, v74\n\t" \
    "v_exp_f32 v75, v75\n\t" \
    "s_waitcnt lgkmcnt(2)\n\t" \
    "v_mfma_f32_32x32x16_bf16 %[oa], %[v0], v[96:99], %[oa]\n\t" \
    "v_cvt_pk_bf16_f32 v100, v72, v73\n\t" \
    "v_cvt_pk_bf16_f32 v101, v74, v75\n\t" \
    "v_exp_f32 v76, v76\n\t" \
    "v_exp_f32 v77, v77\n\t" \
    "v_exp_f32 v78, v78\n\t" \
    "v_exp_f32 v79, v79\n\t" \
    "v_mfma_f32_32x32x16_bf16 %[ob], %[v1], v[108:111], %[ob]\n\t" \
    "ds_read_b128 %[v1], %[av] offset:3776\n\t" \
    "v_cvt_pk_bf16_f32 v102, v76, v77\n\t" \
    "v_cvt_pk_bf16_f32 v103, v78, v79\n\t" \
    "v_exp_f32 v80, v80\n\t" \
    "v_exp_f32 v81, v81\n\t" \
    "v_exp_f32 v82, v82\n\t" \
    "v_exp_f32 v83, v83\n\t" \
    "s_waitcnt lgkmcnt(1)\n\t" \
    "v_mfma_f32_32x32x16_bf16 v[64:79], %[ka], %[qaa], 0\n\t" \
    "v_cvt_pk_bf16_f32 v104, v80, v81\n\t" \
    "v_cvt_pk_bf16_f32 v105, v82, v83\n\t" \
    "v_exp_f32 v84, v84\n\t" \
    "v_exp_f32 v85, v85\n\t" \
    "v_exp_f32 v86, v86\n\t" \
    "v_exp_f32 v87, v87\n\t" \
    "v_mfma_f32_32x32x8bf16_1k v[64:79], %[kb], %[qba], v[64:79]\n\t" \
    "v_cvt_pk_bf16_f32 v106, v84, v85\n\t" \
    "v_cvt_pk_bf16_f32 v107, v86, v87\n\t" \
    "v_exp_f32 v88, v88\n\t" \
    "v_exp_f32 v89, v89\n\t" \
    "v_exp_f32 v90, v90\n\t" \
    "v_exp_f32 v91, v91\n\t" \
    "s_waitcnt lgkmcnt(0)\n\t" \
    "v_mfma_f32_32x32x16_bf16 %[oa], %[v1], v[100:103], %[oa]\n\t" \
    "v_cvt_pk_bf16_f32 v108, v88, v89\n\t" \
    "v_cvt_pk_bf16_f32 v109, v90, v91\n\t" \
    "v_exp_f32 v92, v92\n\t" \
    "v_exp_f32 v93, v93\n\t" \
    "v_exp_f32 v94, v94\n\t" \
    "v_exp_f32 v95, v95\n\t" \
    "ds_write_b32 %[awr], %[vit] offset:16\n\t" \
    "v_mfma_f32_32x32x16_bf16 %[ob], %[v0], v[104:107], %[ob]\n\t" \
    "ds_read_b128 %[v0], %[av] offset:6144\n\t" \
    "v_cvt_pk_bf16_f32 v110, v92, v93\n\t" \
    "v_cvt_pk_bf16_f32 v111, v94, v95\n\t" \
    "v_exp_f32 v64, v64\n\t" \
    "v_exp_f32 v65, v65\n\t" \
    "v_exp_f32 v66, v66\n\t" \
    "v_exp_f32 v67, v67\n\t" \
    "v_mfma_f32_32x32x16_bf16 v[80:95], %[ka], %[qab], 0\n\t" \
    "ds_read_b128 %[ka], %[ak] offset:9216\n\t" \
    "v_cvt_pk_bf16_f32 v96, v64, v65\n\t" \
    "v_cvt_pk_bf16_f32 v97, v66, v67\n\t" \
    "v_exp_f32 v68, v68\n\t" \
    "v_exp_f32 v69, v69\n\t" \
    "v_exp_f32 v70, v70\n\t" \
    "v_exp_f32 v71, v71\n\t" \
    "v_mfma_f32_32x32x8bf16_1k v[80:95], %[kb], %[qbb], v[80:95]\n\t" \
    "ds_read_b64 %[kb], %[akb] offset:9216\n\t" \
    "v_cvt_pk_bf16_f32 v98, v68, v69\n\t" \
    "v_cvt_pk_bf16_f32 v99, v70, v71\n\t" \
    "v_exp_f32 v72, v72\n\t" \
    "v_exp_f32 v73, v73\n\t" \
    "v_exp_f32 v74, v74\n\t" \
    "v_exp_f32 v75, v75\n\t" \
    "s_waitcnt lgkmcnt(2)\n\t" \
    "v_mfma_f32_32x32x16_bf16 %[oa], %[v0], v[96:99], %[oa]\n\t" \
    "v_cvt_pk_bf16_f32 v100, v72, v73\n\t" \
    "v_cvt_pk_bf16_f32 v101, v74, v75\n\t" \
    "v_exp_f32 v76, v76\n\t" \
    "v_exp_f32 v77, v77\n\t" \
    "v_exp_f32 v78, v78\n\t" \
    "v_exp_f32 v79, v79\n\t" \
    "v_mfma_f32_32x32x16_bf16 %[ob], %[v1], v[108:111], %[ob]\n\t" \
    "ds_read_b128 %[v1], %[av] offset:6848\n\t" \
    "v_cvt_pk_bf16_f32 v102, v76, v77\n\t" \
    "v_cvt_pk_bf16_f32 v103, v78, v79\n\t" \
    "v_exp_f32 v80, v80\n\t" \
    "v_exp_f32 v81, v81\n\t" \
    "v_exp_f32 v82, v82\n\t" \
    "v_exp_f32 v83, v83\n\t" \
    "s_waitcnt lgkmcnt(1)\n\t" \
    "v_mfma_f32_32x32x16_bf16 v[64:79], %[ka], %[qaa], 0\n\t" \
    "ds_read_b32 %[vpoll], %[ard] offset:16\n\t" \
    "v_cvt_pk_bf16_f32 v104, v80, v81\n\t" \
    "v_cvt_pk_bf16_f32 v105, v82, v83\n\t" \
    "v_exp_f32 v84, v84\n\t" \
    "v_exp_f32 v85, v85\n\t" \
    "v_exp_f32 v86, v86\n\t" \
    "v_exp_f32 v87, v87\n\t" \
    "v_mfma_f32_32x32x8bf16_1k v[64:79], %[kb], %[qba], v[64:79]\n\t" \
    "v_cvt_pk_bf16_f32 v106, v84, v85\n\t" \
    "v_cvt_pk_bf16_f32 v107, v86, v87\n\t" \
    "v_exp_f32 v88, v88\n\t" \
    "v_exp_f32 v89, v89\n\t" \
    "v_exp_f32 v90, v90\n\t" \
    "v_exp_f32 v91, v91\n\t" \
    "s_waitcnt lgkmcnt(0)\n\t" \
    "v_mfma_f32_32x32x16_bf16 %[oa], %[v1], v[100:103], %[oa]\n\t" \
    "v_cvt_pk_bf16_f32 v108, v88, v89\n\t" \
    "v_cvt_pk_bf16_f32 v109, v90, v91\n\t" \
    "v_exp_f32 v92, v92\n\t" \
    "v_exp_f32 v93, v93\n\t" \
    "v_exp_f32 v94, v94\n\t" \
    "v_exp_f32 v95, v95\n\t" \
    "v_cmp_lt_u32 vcc, %[vpoll], %[vit]\n\t" \
    "s_cbranch_vccz 14f\n\t" \
    "s_mov_b32 %[sspin], 0\n\t" \
    "15:\n\t" \
    "s_sleep 1\n\t" \
    "ds_read_b32 %[vpoll], %[ard] offset:16\n\t" \
    "s_add_u32 %[sspin], %[sspin], 1\n\t" \
    "s_waitcnt lgkmcnt(0)\n\t" \
    "s_cmp_gt_u32 %[sspin], 0x40000\n\t" \
    "s_cbranch_scc1 16f\n\t" \
    "v_cmp_lt_u32 vcc, %[vpoll], %[vit]\n\t" \
    "s_cbranch_vccnz 15b\n\t" \
    "16:\n\t" \
    "s_cmp_eq_u32 %[sc], %[slast]\n\t" \
    "14:\n\t" \
    "v_mfma_f32_32x32x16_bf16 %[ob], %[v0], v[104:107], %[ob]\n\t" \
    "ds_read_b128 %[v0], %[av] offset:9216\n\t" \
    "v_cvt_pk_bf16_f32 v110, v92, v93\n\t" \
    "v_cvt_pk_bf16_f32 v111, v94, v95\n\t" \
    "v_exp_f32 v64, v64\n\t" \
    "v_exp_f32 v65, v65\n\t" \
    "v_exp_f32 v66, v66\n\t" \
    "v_exp_f32 v67, v67\n\t" \
    "v_mfma_f32_32x32x16_bf16 v[80:95], %[ka], %[qab], 0\n\t" \
    "ds_read_b128 %[ka], %[ak] offset:24576\n\t" \
    "v_cvt_pk_bf16_f32 v96, v64, v65\n\t" \
    "v_cvt_pk_bf16_f32 v97, v66, v67\n\t" \
    "v_exp_f32 v68, v68\n\t" \
    "v_exp_f32 v69, v69\n\t" \
    "v_exp_f32 v70, v70\n\t" \
    "v_exp_f32 v71, v71\n\t" \
    "v_mfma_f32_32x32x8bf16_1k v[80:95], %[kb], %[qbb], v[80:95]\n\t" \
    "ds_read_b64 %[kb], %[akb] offset:24576\n\t" \
    "v_cvt_pk_bf16_f32 v98, v68, v69\n\t" \
    "v_cvt_pk_bf16_f32 v99, v70, v71\n\t" \
    "v_exp_f32 v72, v72\n\t" \
    "v_exp_f32 v73, v73\n\t" \
    "v_exp_f32 v74, v74\n\t" \
    "v_exp_f32 v75, v75\n\t" \
    "s_waitcnt lgkmcnt(2)\n\t" \
    "v_mfma_f32_32x32x16_bf16 %[oa], %[v0], v[96:99], %[oa]\n\t" \
    "v_cvt_pk_bf16_f32 v100, v72, v73\n\t" \
    "v_cvt_pk_bf16_f32 v101, v74, v75\n\t" \
    "v_exp_f32 v76, v76\n\t" \
    "v_exp_f32 v77, v77\n\t" \
    "v_exp_f32 v78, v78\n\t" \
    "v_exp_f32 v79, v79\n\t" \
    "v_mfma_f32_32x32x16_bf16 %[ob], %[v1], v[108:111], %[ob]\n\t" \
    "ds_read_b128 %[v1], %[av] offset:9920\n\t" \
    "v_cvt_pk_bf16_f32 v102, v76, v77\n\t" \
    "v_cvt_pk_bf16_f32 v103, v78, v79\n\t" \
    "v_exp_f32 v80, v80\n\t" \
    "v_exp_f32 v81, v81\n\t" \
    "v_exp_f32 v82, v82\n\t" \
    "v_exp_f32 v83, v83\n\t" \
    "s_waitcnt lgkmcnt(1)\n\t" \
    "v_mfma_f32_32x32x16_bf16 v[64:79], %[ka], %[qaa], 0\n\t" \
    "v_cvt_pk_bf16_f32 v104, v80, v81\n\t" \
    "v_cvt_pk_bf16_f32 v105, v82, v83\n\t" \
    "v_exp_f32 v84, v84\n\t" \
    "v_exp_f32 v85, v85\n\t" \
    "v_exp_f32 v86, v86\n\t" \
    "v_exp_f32 v87, v87\n\t" \
    "v_mfma_f32_32x32x8bf16_1k v[64:79], %[kb], %[qba], v[64:79]\n\t" \
    "v_cvt_pk_bf16_f32 v106, v84, v85\n\t" \
    "v_cvt_pk_bf16_f32 v107, v86, v87\n\t" \
    "v_exp_f32 v88, v88\n\t" \
    "v_exp_f32 v89, v89\n\t" \
    "v_exp_f32 v90, v90\n\t" \
    "v_exp_f32 v91, v91\n\t" \
    "s_waitcnt lgkmcnt(0)\n\t" \
    "v_mfma_f32_32x32x16_bf16 %[oa], %[v1], v[100:103], %[oa]\n\t" \
    "v_cvt_pk_bf16_f32 v108, v88, v89\n\t" \
    "v_cvt_pk_bf16_f32 v109, v90, v91\n\t" \
    "v_exp_f32 v92, v92\n\t" \
    "v_exp_f32 v93, v93\n\t" \
    "v_exp_f32 v94, v94\n\t" \
    "v_exp_f32 v95, v95\n\t" \
    "s_waitcnt vmcnt(0)\n\t" \
    "ds_write_b32 %[awr], %[vit] offset:32\n\t" \
    "v_mfma_f32_32x32x16_bf16 %[ob], %[v0], v[104:107], %[ob]\n\t" \
    "ds_read_b128 %[v0], %[av] offset:24576\n\t" \
    "v_cvt_pk_bf16_f32 v110, v92, v93\n\t" \
    "v_cvt_pk_bf16_f32 v111, v94, v95\n\t" \
    "v_exp_f32 v64, v64\n\t" \
    "v_exp_f32 v65, v65\n\t" \
    "v_exp_f32 v66, v66\n\t" \
    "v_exp_f32 v67, v67\n\t" \
    "v_mfma_f32_32x32x16_bf16 v[80:95], %[ka], %[qab], 0\n\t" \
    "ds_read_b128 %[ka], %[ak] offset:27648\n\t" \
    "v_cvt_pk_bf16_f32 v96, v64, v65\n\t" \
    "v_cvt_pk_bf16_f32 v97, v66, v67\n\t" \
    "v_exp_f32 v68, v68\n\t" \
    "v_exp_f32 v69, v69\n\t" \
    "v_exp_f32 v70, v70\n\t" \
    "v_exp_f32 v71, v71\n\t" \
    "v_mfma_f32_32x32x8bf16_1k v[80:95], %[kb], %[qbb], v[80:95]\n\t" \
    "ds_read_b64 %[kb], %[akb] offset:27648\n\t" \
    "v_cvt_pk_bf16_f32 v98, v68, v69\n\t" \
    "v_cvt_pk_bf16_f32 v99, v70, v71\n\t" \
    "v_exp_f32 v72, v72\n\t" \
    "v_exp_f32 v73, v73\n\t" \
    "v_exp_f32 v74, v74\n\t" \
    "v_exp_f32 v75, v75\n\t" \
    "s_waitcnt lgkmcnt(2)\n\t" \
    "v_mfma_f32_32x32x16_bf16 %[oa], %[v0], v[96:99], %[oa]\n\t" \
    "v_cvt_pk_bf16_f32 v100, v72, v73\n\t" \
    "v_cvt_pk_bf16_f32 v101, v74, v75\n\t" \
    "v_exp_f32 v76, v76\n\t" \
    "v_exp_f32 v77, v77\n\t" \
    "v_exp_f32 v78, v78\n\t" \
    "v_exp_f32 v79, v79\n\t" \
    "v_mfma_f32_32x32x16_bf16 %[ob], %[v1], v[108:111], %[ob]\n\t" \
    "ds_read_b128 %[v1], %[av] offset:25280\n\t" \
    "v_cvt_pk_bf16_f32 v102, v76, v77\n\t" \
    "v_cvt_pk_bf16_f32 v103, v78, v79\n\t" \
    "v_exp_f32 v80, v80\n\t" \
    "v_exp_f32 v81, v81\n\t" \
    "v_exp_f32 v82, v82\n\t" \
    "v_exp_f32 v83, v83\n\t" \
    "s_waitcnt lgkmcnt(1)\n\t" \
    "v_mfma_f32_32x32x16_bf16 v[64:79], %[ka], %[qaa], 0\n\t" \
    "ds_read_b32 %[vpoll], %[ard] offset:32\n\t" \
    "v_cvt_pk_bf16_f32 v104, v80, v81\n\t" \
    "v_cvt_pk_bf16_f32 v105, v82, v83\n\t" \
    "v_exp_f32 v84, v84\n\t" \
    "v_exp_f32 v85, v85\n\t" \
    "v_exp_f32 v86, v86\n\t" \
    "v_exp_f32 v87, v87\n\t" \
    "v_mfma_f32_32x32x8bf16_1k v[64:79], %[kb], %[qba], v[64:79]\n\t" \
    "v_cvt_pk_bf16_f32 v106, v84, v85\n\t" \
    "v_cvt_pk_bf16_f32 v107, v86, v87\n\t" \
    "v_exp_f32 v88, v88\n\t" \
    "v_exp_f32 v89, v89\n\t" \
    "v_exp_f32 v90, v90\n\t" \
    "v_exp_f32 v91, v91\n\t" \
    "s_waitcnt lgkmcnt(0)\n\t" \
    "v_mfma_f32_32x32x16_bf16 %[oa], %[v1], v[100:103], %[oa]\n\t" \
    "v_cvt_pk_bf16_f32 v108, v88, v89\n\t" \
    "v_cvt_pk_bf16_f32 v109, v90, v91\n\t" \
    "v_exp_f32 v92, v92\n\t" \
    "v_exp_f32 v93, v93\n\t" \
    "v_exp_f32 v94, v94\n\t" \
    "v_exp_f32 v95, v95\n\t" \
    "v_cmp_lt_u32 vcc, %[vpoll], %[vit]\n\t" \
    "s_cbranch_vccz 17f\n\t" \
    "s_mov_b32 %[sspin], 0\n\t" \
    "18:\n\t" \
    "s_sleep 1\n\t" \
    "ds_read_b32 %[vpoll], %[ard] offset:32\n\t" \
    "s_add_u32 %[sspin], %[sspin], 1\n\t" \
    "s_waitcnt lgkmcnt(0)\n\t" \
    "s_cmp_gt_u32 %[sspin], 0x40000\n\t" \
    "s_cbranch_scc1 19f\n\t" \
    "v_cmp_lt_u32 vcc, %[vpoll], %[vit]\n\t" \
    "s_cbranch_vccnz 18b\n\t" \
    "19:\n\t" \
    "s_cmp_eq_u32 %[sc], %[slast]\n\t" \
    "17:\n\t" \
    "s_mov_b32 m0, %[sd1]\n\t" \
    "s_cbranch_scc1 20f\n\t" \
    "global_load_lds_dwordx4 %[voff], %[sg] sc1\n\t" \
    "global_load_lds_dwordx4 %[voff], %[sg] offset:1024 sc1\n\t" \
    "global_load_lds_dwordx4 %[voff], %[sg] offset:2048 sc1\n\t" \
    "v_add_u32 %[voff], 12288, %[voff]\n\t" \
    "20:\n\t" \
    "v_mfma_f32_32x32x16_bf16 %[ob], %[v0], v[104:107], %[ob]\n\t" \
    "ds_read_b128 %[v0], %[av] offset:27648\n\t" \
    "v_cvt_pk_bf16_f32 v110, v92, v93\n\t" \
    "v_cvt_pk_bf16_f32 v111, v94, v95\n\t" \
    "v_exp_f32 v64, v64\n\t" \
    "v_exp_f32 v65, v65\n\t" \
    "v_exp_f32 v66, v66\n\t" \
    "v_exp_f32 v67, v67\n\t" \
    "v_mfma_f32_32x32x16_bf16 v[80:95], %[ka], %[qab], 0\n\t" \
    "ds_read_b128 %[ka], %[ak] offset:30720\n\t" \
    "v_cvt_pk_bf16_f32 v96, v64, v65\n\t" \
    "v_cvt_pk_bf16_f32 v97, v66, v67\n\t" \
    "v_exp_f32 v68, v68\n\t" \
    "v_exp_f32 v69, v69\n\t" \
    "v_exp_f32 v70, v70\n\t" \
    "v_exp_f32 v71, v71\n\t" \
    "v_mfma_f32_32x32x8bf16_1k v[80:95], %[kb], %[qbb], v[80:95]\n\t" \
    "ds_read_b64 %[kb], %[akb] offset:30720\n\t" \
    "v_cvt_pk_bf16_f32 v98, v68, v69\n\t" \
    "v_cvt_pk_bf16_f32 v99, v70, v71\n\t" \
    "v_exp_f32 v72, v72\n\t" \
    "v_exp_f32 v73, v73\n\t" \
    "v_exp_f32 v74, v74\n\t" \
    "v_exp_f32 v75, v75\n\t" \
    "s_waitcnt lgkmcnt(2)\n\t" \
    "v_mfma_f32_32x32x16_bf16 %[oa], %[v0], v[96:99], %[oa]\n\t" \
    "v_cvt_pk_bf16_f32 v100, v72, v73\n\t" \
    "v_cvt_pk_bf16_f32 v101, v74, v75\n\t" \
    "v_exp_f32 v76, v76\n\t" \
    "v_exp_f32 v77, v77\n\t" \
    "v_exp_f32 v78, v78\n\t" \
    "v_exp_f32 v79, v79\n\t" \
    "v_mfma_f32_32x32x16_bf16 %[ob], %[v1], v[108:111], %[ob]\n\t" \
    "ds_read_b128 %[v1], %[av] offset:28352\n\t" \
    "v_cvt_pk_bf16_f32 v102, v76, v77\n\t" \
    "v_cvt_pk_bf16_f32 v103, v78, v79\n\t" \
    "v_exp_f32 v80, v80\n\t" \
    "v_exp_f32 v81, v81\n\t" \
    "v_exp_f32 v82, v82\n\t" \
    "v_exp_f32 v83, v83\n\t" \
    "s_waitcnt lgkmcnt(1)\n\t" \
    "v_mfma_f32_32x32x16_bf16 v[64:79], %[ka], %[qaa], 0\n\t" \
    "v_cvt_pk_bf16_f32 v104, v80, v81\n\t" \
    "v_cvt_pk_bf16_f32 v105, v82, v83\n\t" \
    "v_exp_f32 v84, v84\n\t" \
    "v_exp_f32 v85, v85\n\t" \
    "v_exp_f32 v86, v86\n\t" \
    "v_exp_f32 v87, v87\n\t" \
    "v_mfma_f32_32x32x8bf16_1k v[64:79], %[kb], %[qba], v[64:79]\n\t" \
    "v_cvt_pk_bf16_f32 v106, v84, v85\n\t" \
    "v_cvt_pk_bf16_f32 v107, v86, v87\n\t" \
    "v_exp_f32 v88, v88\n\t" \
    "v_exp_f32 v89, v89\n\t" \
    "v_exp_f32 v90, v90\n\t" \
    "v_exp_f32 v91, v91\n\t" \
    "s_waitcnt lgkmcnt(0)\n\t" \
    "v_mfma_f32_32x32x16_bf16 %[oa], %[v1], v[100:103], %[oa]\n\t" \
    "v_cvt_pk_bf16_f32 v108, v88, v89\n\t" \
    "v_cvt_pk_bf16_f32 v109, v90, v91\n\t" \
    "v_exp_f32 v92, v92\n\t" \
    "v_exp_f32 v93, v93\n\t" \
    "v_exp_f32 v94, v94\n\t" \
    "v_exp_f32 v95, v95\n\t" \
    "ds_write_b32 %[awr], %[vit] offset:48\n\t" \
    "v_mfma_f32_32x32x16_bf16 %[ob], %[v0], v[104:107], %[ob]\n\t" \
    "ds_read_b128 %[v0], %[av] offset:30720\n\t" \
    "v_cvt_pk_bf16_f32 v110, v92, v93\n\t" \
    "v_cvt_pk_bf16_f32 v111, v94, v95\n\t" \
    "v_exp_f32 v64, v64\n\t" \
    "v_exp_f32 v65, v65\n\t" \
    "v_exp_f32 v66, v66\n\t" \
    "v_exp_f32 v67, v67\n\t" \
    "v_mfma_f32_32x32x16_bf16 v[80:95], %[ka], %[qab], 0\n\t" \
    "ds_read_b128 %[ka], %[ak] offset:33792\n\t" \
    "v_cvt_pk_bf16_f32 v96, v64, v65\n\t" \
    "v_cvt_pk_bf16_f32 v97, v66, v67\n\t" \
    "v_exp_f32 v68, v68\n\t" \
    "v_exp_f32 v69, v69\n\t" \
    "v_exp_f32 v70, v70\n\t" \
    "v_exp_f32 v71, v71\n\t" \
    "v_mfma_f32_32x32x8bf16_1k v[80:95], %[kb], %[qbb], v[80:95]\n\t" \
    "ds_read_b64 %[kb], %[akb] offset:33792\n\t" \
    "v_cvt_pk_bf16_f32 v98, v68, v69\n\t" \
    "v_cvt_pk_bf16_f32 v99, v70, v71\n\t" \
    "v_exp_f32 v72, v72\n\t" \
    "v_exp_f32 v73, v73\n\t" \
    "v_exp_f32 v74, v74\n\t" \
    "v_exp_f32 v75, v75\n\t" \
    "s_waitcnt lgkmcnt(2)\n\t" \
    "v_mfma_f32_32x32x16_bf16 %[oa], %[v0], v[96:99], %[oa]\n\t" \
    "v_cvt_pk_bf16_f32 v100, v72, v73\n\t" \
    "v_cvt_pk_bf16_f32 v101, v74, v75\n\t" \
    "v_exp_f32 v76, v76\n\t" \
    "v_exp_f32 v77, v77\n\t" \
    "v_exp_f32 v78, v78\n\t" \
    "v_exp_f32 v79, v79\n\t" \
    "v_mfma_f32_32x32x16_bf16 %[ob], %[v1], v[108:111], %[ob]\n\t" \
    "ds_read_b128 %[v1], %[av] offset:31424\n\t" \
    "v_cvt_pk_bf16_f32 v102, v76, v77\n\t" \
    "v_cvt_pk_bf16_f32 v103, v78, v79\n\t" \
    "v_exp_f32 v80, v80\n\t" \
    "v_exp_f32 v81, v81\n\t" \
    "v_exp_f32 v82, v82\n\t" \
    "v_exp_f32 v83, v83\n\t" \
    "s_waitcnt lgkmcnt(1)\n\t" \
    "v_mfma_f32_32x32x16_bf16 v[64:79], %[ka], %[qaa], 0\n\t" \
    "ds_read_b32 %[vpoll], %[ard] offset:48\n\t" \
    "v_cvt_pk_bf16_f32 v104, v80, v81\n\t" \
    "v_cvt_pk_bf16_f32 v105, v82, v83\n\t" \
    "v_exp_f32 v84, v84\n\t" \
    "v_exp_f32 v85, v85\n\t" \
    "v_exp_f32 v86, v86\n\t" \
    "v_exp_f32 v87, v87\n\t" \
    "v_mfma_f32_32x32x8bf16_1k v[64:79], %[kb], %[qba], v[64:79]\n\t" \
    "v_cvt_pk_bf16_f32 v106, v84, v85\n\t" \
    "v_cvt_pk_bf16_f32 v107, v86, v87\n\t" \
    "v_exp_f32 v88, v88\n\t" \
    "v_exp_f32 v89, v89\n\t" \
    "v_exp_f32 v90, v90\n\t" \
    "v_exp_f32 v91, v91\n\t" \
    "s_waitcnt lgkmcnt(0)\n\t" \
    "v_mfma_f32_32x32x16_bf16 %[oa], %[v1], v[100:103], %[oa]\n\t" \
    "v_cvt_pk_bf16_f32 v108, v88, v89\n\t" \
    "v_cvt_pk_bf16_f32 v109, v90, v91\n\t" \
    "v_exp_f32 v92, v92\n\t" \
    "v_exp_f32 v93, v93\n\t" \
    "v_exp_f32 v94, v94\n\t" \
    "v_exp_f32 v95, v95\n\t" \
    "v_cmp_lt_u32 vcc, %[vpoll], %[vit]\n\t" \
    "s_cbranch_vccz 21f\n\t" \
    "s_mov_b32 %[sspin], 0\n\t" \
    "22:\n\t" \
    "s_sleep 1\n\t" \
    "ds_read_b32 %[vpoll], %[ard] offset:48\n\t" \
    "s_add_u32 %[sspin], %[sspin], 1\n\t" \
    "s_waitcnt lgkmcnt(0)\n\t" \
    "s_cmp_gt_u32 %[sspin], 0x40000\n\t" \
    "s_cbranch_scc1 23f\n\t" \
    "v_cmp_lt_u32 vcc, %[vpoll], %[vit]\n\t" \
    "s_cbranch_vccnz 22b\n\t" \
    "23:\n\t" \
    "s_cmp_eq_u32 %[sc], %[slast]\n\t" \
    "21:\n\t" \
    "v_mfma_f32_32x32x16_bf16 %[ob], %[v0], v[104:107], %[ob]\n\t" \
    "ds_read_b128 %[v0], %[av] offset:33792\n\t" \
    "v_cvt_pk_bf16_f32 v110, v92, v93\n\t" \
    "v_cvt_pk_bf16_f32 v111, v94, v95\n\t" \
    "v_exp_f32 v64, v64\n\t" \
    "v_exp_f32 v65, v65\n\t" \
    "v_exp_f32 v66, v66\n\t" \
    "v_exp_f32 v67, v67\n\t" \
    "v_mfma_f32_32x32x16_bf16 v[80:95], %[ka], %[qab], 0\n\t" \
    "ds_read_b128 %[ka], %[ak] offset:0\n\t" \
    "v_cvt_pk_bf16_f32 v96, v64, v65\n\t" \
    "v_cvt_pk_bf16_f32 v97, v66, v67\n\t" \
    "v_exp_f32 v68, v68\n\t" \
    "v_exp_f32 v69, v69\n\t" \
    "v_exp_f32 v70, v70\n\t" \
    "v_exp_f32 v71, v71\n\t" \
    "v_mfma_f32_32x32x8bf16_1k v[80:95], %[kb], %[qbb], v[80:95]\n\t" \
    "ds_read_b64 %[kb], %[akb] offset:0\n\t" \
    "v_cvt_pk_bf16_f32 v98, v68, v69\n\t" \
    "v_cvt_pk_bf16_f32 v99, v70, v71\n\t" \
    "v_exp_f32 v72, v72\n\t" \
    "v_exp_f32 v73, v73\n\t" \
    "v_exp_f32 v74, v74\n\t" \
    "v_exp_f32 v75, v75\n\t" \
    "s_waitcnt lgkmcnt(2)\n\t" \
    "v_mfma_f32_32x32x16_bf16 %[oa], %[v0], v[96:99], %[oa]\n\t" \
    "v_cvt_pk_bf16_f32 v100, v72, v73\n\t" \
    "v_cvt_pk_bf16_f32 v101, v74, v75\n\t" \
    "v_exp_f32 v76, v76\n\t" \
    "v_exp_f32 v77, v77\n\t" \
    "v_exp_f32 v78, v78\n\t" \
    "v_exp_f32 v79, v79\n\t" \
    "v_mfma_f32_32x32x16_bf16 %[ob], %[v1], v[108:111], %[ob]\n\t" \
    "ds_read_b128 %[v1], %[av] offset:34496\n\t" \
    "v_cvt_pk_bf16_f32 v102, v76, v77\n\t" \
    "v_cvt_pk_bf16_f32 v103, v78, v79\n\t" \
    "v_exp_f32 v80, v80\n\t" \
    "v_exp_f32 v81, v81\n\t" \
    "v_exp_f32 v82, v82\n\t" \
    "v_exp_f32 v83, v83\n\t" \
    "s_waitcnt lgkmcnt(1)\n\t" \
    "v_mfma_f32_32x32x16_bf16 v[64:79], %[ka], %[qaa], 0\n\t" \
    "v_cvt_pk_bf16_f32 v104, v80, v81\n\t" \
    "v_cvt_pk_bf16_f32 v105, v82, v83\n\t" \
    "v_exp_f32 v84, v84\n\t" \
    "v_exp_f32 v85, v85\n\t" \
    "v_exp_f32 v86, v86\n\t" \
    "v_exp_f32 v87, v87\n\t" \
    "v_mfma_f32_32x32x8bf16_1k v[64:79], %[kb], %[qba], v[64:79]\n\t" \
    "v_cvt_pk_bf16_f32 v106, v84, v85\n\t" \
    "v_cvt_pk_bf16_f32 v107, v86, v87\n\t" \
    "v_exp_f32 v88, v88\n\t" \
    "v_exp_f32 v89, v89\n\t" \
    "v_exp_f32 v90, v90\n\t" \
    "v_exp_f32 v91, v91\n\t" \
    "s_waitcnt lgkmcnt(0)\n\t" \
    "v_mfma_f32_32x32x16_bf16 %[oa], %[v1], v[100:103], %[oa]\n\t" \
    "v_cvt_pk_bf16_f32 v108, v88, v89\n\t" \
    "v_cvt_pk_bf16_f32 v109, v90, v91\n\t" \
    "v_exp_f32 v92, v92\n\t" \
    "v_exp_f32 v93, v93\n\t" \
    "v_exp_f32 v94, v94\n\t" \
    "v_exp_f32 v95, v95\n\t" \
    "v_add_u32 %[vit], 1, %[vit]\n\t" \
    "s_add_u32 %[sc], %[sc], 1\n\t" \
    "s_cmp_le_u32 %[sc], %[slast]\n\t" \
    "s_cbranch_scc1 1b\n\t" \
    "v_cvt_pk_bf16_f32 v110, v92, v93\n\t" \
    "v_cvt_pk_bf16_f32 v111, v94, v95\n\t" \
    "s_nop 1\n\t" \
    "v_mfma_f32_32x32x16_bf16 %[ob], %[v0], v[104:107], %[ob]\n\t" \
    "v_mfma_f32_32x32x16_bf16 %[ob], %[v1], v[108:111], %[ob]\n\t" \
    "s_mov_b32 m0, %[sm0]\n\t" \
    "s_nop 15\n\t"
#define ATTN_ASM_CLOB "v64", "v65", "v66", "v67", "v68", "v69", "v70", "v71", "v72", "v73", "v74", "v75", "v76", "v77", "v78", "v79", "v80", "v81", "v82", "v83", "v84", "v85", "v86", "v87", "v88", "v89", "v90", "v91", "v92", "v93", "v94", "v95", "v96", "v97", "v98", "v99", "v100", "v101", "v102", "v103", "v104", "v105", "v106", "v107", "v108", "v109", "v110", "v111", "vcc"

DI bf16x8 ldtab(__amdgpu_buffer_rsrc_t rs, unsigned voff, unsigned soff) { return __builtin_bit_cast(bf16x8, __builtin_amdgcn_raw_buffer_load_b128(rs, voff, soff, 0)); }
struct Params { const u16 *x, *wq, *wk, *wv, *wo, *wr, *wi, *gw; char* ws; u16* out; };

template <int SG> DI void cmul2(f32x4& re, f32x4& im, int h, f32x2 c, f32x2 s) {
    const f32x2 a = {re[2 * h], re[2 * h + 1]}, b = {im[2 * h], im[2 * h + 1]};
    const f32x2 t = b * s, u = a * s;
    const f32x2 r = SG > 0 ? __builtin_elementwise_fma(a, c, -t) : __builtin_elementwise_fma(a, c, t);
    const f32x2 q = SG > 0 ? __builtin_elementwise_fma(b, c, u) : __builtin_elementwise_fma(b, c, -u);
    re[2 * h] = r[0]; re[2 * h + 1] = r[1]; im[2 * h] = q[0]; im[2 * h + 1] = q[1];
}
DI bf16x8 pk8(const f32x4& a, const f32x4& b) { u32x4 u = {pk2(a[0], a[1]), pk2(a[2], a[3]), pk2(b[0], b[1]), pk2(b[2], b[3])}; return __builtin_bit_cast(bf16x8, u); }
struct DhtRegs { bf16x8 D[2], F[2][3]; f32x4 t[4]; };
template <int NR> DI void dht(const LAS char* const (&ad)[NR][2][2], const DhtRegs& R, f32x4 (&hp)[NR][2], f32x4 (&hm)[NR][2]) {
    const f32x4 z4 = {0, 0, 0, 0};
    bf16x8 br[NR], bi[NR];
    {
        bf16x8 af[NR][2];
#pragma unroll
        for (int rw = 0; rw < NR; ++rw)
#pragma unroll
            for (int mt = 0; mt < 2; ++mt) af[rw][mt] = cat8(trd(ad[rw][mt][0]), trd(ad[rw][mt][1]));
        f32x4 tre[NR][2], tim[NR][2];
#pragma unroll
        for (int rw = 0; rw < NR; ++rw)
#pragma unroll
            for (int mt = 0; mt < 2; ++mt) { tre[rw][mt] = MFMA16(af[rw][mt], R.D[0], z4); tim[rw][mt] = MFMA16(af[rw][mt], R.D[1], z4); }
#pragma unroll
        for (int rw = 0; rw < NR; ++rw) {
#pragma unroll
            for (int i = 0; i < 4; ++i) cmul2<-1>(tre[rw][i >> 1], tim[rw][i >> 1], i & 1, (f32x2){R.t[i][0], R.t[i][1]}, (f32x2){R.t[i][2], R.t[i][3]});
            br[rw] = pk8(tre[rw][0], tre[rw][1]); bi[rw] = pk8(tim[rw][0], tim[rw][1]); }
    }
#pragma unroll
    for (int rw = 0; rw < NR; ++rw)
#pragma unroll
        for (int mt = 0; mt < 2; ++mt) { hp[rw][mt] = MFMA16(R.F[mt][0], br[rw], z4); hm[rw][mt] = MFMA16(R.F[mt][0], bi[rw], z4); }
#pragma unroll
    for (int rw = 0; rw < NR; ++rw)
#pragma unroll
        for (int mt = 0; mt < 2; ++mt) { hp[rw][mt] = MFMA16(R.F[mt][1], bi[rw], hp[rw][mt]); hm[rw][mt] = MFMA16(R.F[mt][2], br[rw], hm[rw][mt]); }
#pragma unroll
    for (int rw = 0; rw < NR; ++rw)
#pragma unroll
        for (int mt = 0; mt < 2; ++mt) { const f32x4 re = hp[rw][mt], im = hm[rw][mt]; hp[rw][mt] = re - im; hm[rw][mt] = re + im; }
}
DI void dht_load(DhtRegs& R, int lane) {
    const bf16x8* f32 = (const bf16x8*)&B2T.f32[0][0][0][0]; const bf16x8* d20 = (const bf16x8*)&B2T.d20[0][0][0];
#pragma unroll
    for (int a = 0; a < 2; ++a) { R.D[a] = d20[a * 64 + lane];
#pragma unroll
        for (int t = 0; t < 3; ++t) R.F[a][t] = f32[(a * 3 + t) * 64 + lane]; }
#pragma unroll
    for (int i = 0; i < 4; ++i) R.t[i] = *(const f32x4*)&B2T.twr[i][lane][0];
}
template <int U> DI void dht_put(LAS char* img, const f32x4 (&hp0)[2], const f32x4 (&hp1)[2], const f32x4 (&hm0)[2], const f32x4 (&hm1)[2], float s0, float s1, int lane) {
    const int g = lane >> 4, cl = lane & 15;
    LAS char* p1 = img + (80 * g + cl) * U; LAS char* p2 = img + (260 - 80 * g - cl) * U;
    if (cl <= 10) {
#pragma unroll
        for (int i = 0; i < 8; ++i) *(LAS unsigned*)(p1 + 20 * (16 * (i >> 2) + (i & 3)) * U) = pk2(hp0[i >> 2][i & 3] * s0, hp1[i >> 2][i & 3] * s1); }
    if (cl >= 1 && cl <= 9) {
#pragma unroll
        for (int i = 0; i < 8; ++i) *(LAS unsigned*)(p2 + 20 * (19 - 16 * (i >> 2) - (i & 3)) * U) = pk2(hm0[i >> 2][i & 3] * s0, hm1[i >> 2][i & 3] * s1); }
}
struct B2Regs { bf16x8 A[2][3], F[2][3]; f32x4 t1[4], t2[4]; };

template <int NC> DI void b2_columns(const Params& p, const int (&jj)[2], int cb, int nt, int lane, LAS char* smem_, ldsw_t C, const B2Regs& R, const u32x4 (&wv)[2][2], unsigned& svc_t) {
    LAS char* smem = smem_ + cb * 8192;
    const int g = lane >> 4, cl = lane & 15;
    const f32x4 z4 = {0, 0, 0, 0};
    f32x4 tre[NC][2] = {}, tim[NC][2] = {};
    {
        const int a0 = rswz(cl, g), a1 = rswz(cl, 4 + g);
#pragma unroll
        for (int hf = 0; hf < 2; ++hf) {
            bf16x8 zp[NC][2], zm[NC][2];
#pragma unroll
            for (int ci = 0; ci < NC; ++ci)
#pragma unroll
                for (int mt = 0; mt < 2; ++mt) { const LAS char* in = smem + L_B2I + ci * 8192 + mt * 2048 + (hf ? a1 : a0); zp[ci][mt] = ldsr128(in); zm[ci][mt] = ldsr128(in + 4096); }
#pragma unroll
            for (int ci = 0; ci < NC; ++ci)
#pragma unroll
                for (int mt = 0; mt < 2; ++mt) { tre[ci][mt] = MFMA16(zp[ci][mt], R.A[hf][0], tre[ci][mt]); tim[ci][mt] = MFMA16(zp[ci][mt], R.A[hf][2], tim[ci][mt]); }
#pragma unroll
            for (int ci = 0; ci < NC; ++ci)
#pragma unroll
                for (int mt = 0; mt < 2; ++mt) { tre[ci][mt] = MFMA16(zm[ci][mt], R.A[hf][1], tre[ci][mt]); tim[ci][mt] = MFMA16(zm[ci][mt], R.A[hf][0], tim[ci][mt]); }
        }
    }
    bf16x8 br[NC], bi[NC];
#pragma unroll
    for (int ci = 0; ci < NC; ++ci) {
#pragma unroll
        for (int i = 0; i < 4; ++i) cmul2<-1>(tre[ci][i >> 1], tim[ci][i >> 1], i & 1, (f32x2){R.t1[i][0], R.t1[i][1]}, (f32x2){R.t1[i][2], R.t1[i][3]});
        br[ci] = pk8(tre[ci][0], tre[ci][1]); bi[ci] = pk8(tim[ci][0], tim[ci][1]); }
    f32x4 zre[NC][2], zim[NC][2];
#pragma unroll
    for (int ci = 0; ci < NC; ++ci)
#pragma unroll
        for (int mt = 0; mt < 2; ++mt) { zre[ci][mt] = MFMA16(R.F[mt][0], br[ci], z4); zim[ci][mt] = MFMA16(R.F[mt][0], bi[ci], z4); }
#pragma unroll
    for (int ci = 0; ci < NC; ++ci)
#pragma unroll
        for (int mt = 0; mt < 2; ++mt) { zre[ci][mt] = MFMA16(R.F[mt][1], bi[ci], zre[ci][mt]); zim[ci][mt] = MFMA16(R.F[mt][2], br[ci], zim[ci][mt]); }
    bf16x8 ar[NC], ai[NC];
#pragma unroll
    for (int ci = 0; ci < NC; ++ci) {
#pragma unroll
        for (int i = 0; i < 4; ++i) { const unsigned w0 = wv[ci][i >> 1][2 * (i & 1)], w1 = wv[ci][i >> 1][2 * (i & 1) + 1];
            cmul2<1>(zre[ci][i >> 1], zim[ci][i >> 1], i & 1, (f32x2){lo2f(w0), lo2f(w1)}, (f32x2){hi2f(w0), hi2f(w1)}); }
        ar[ci] = pk8(zre[ci][0], zre[ci][1]); ai[ci] = pk8(zim[ci][0], zim[ci][1]); }
    f32x4 ure[NC][2], uim[NC][2];
#pragma unroll
    for (int ci = 0; ci < NC; ++ci)
#pragma unroll
        for (int n3 = 0; n3 < 2; ++n3) { ure[ci][n3] = MFMA16(ar[ci], R.F[n3][0], z4); uim[ci][n3] = MFMA16(ai[ci], R.F[n3][0], z4); }
#pragma unroll
    for (int ci = 0; ci < NC; ++ci)
#pragma unroll
        for (int n3 = 0; n3 < 2; ++n3) { ure[ci][n3] = MFMA16(ai[ci], R.F[n3][2], ure[ci][n3]); uim[ci][n3] = MFMA16(ar[ci], R.F[n3][1], uim[ci][n3]); }
    LAS char* xw = smem + L_B2X + cl * 256 + (((2 * nt + (g >> 1)) ^ (cl & 7)) << 4) + (g & 1) * 8;
#pragma unroll
    for (int ci = 0; ci < NC; ++ci) {
#pragma unroll
        for (int i = 0; i < 4; ++i) cmul2<1>(ure[ci][i >> 1], uim[ci][i >> 1], i & 1, (f32x2){R.t2[i][0], R.t2[i][1]}, (f32x2){R.t2[i][2], R.t2[i][3]});
#pragma unroll
        for (int n3 = 0; n3 < 2; ++n3) { LAS char* d = xw + ci * 8192 + n3 * 4096;
            *(LAS u32x2*)d = (u32x2){pk2(ure[ci][n3][0], ure[ci][n3][1]), pk2(ure[ci][n3][2], ure[ci][n3][3])};
            *(LAS u32x2*)(d + 128) = (u32x2){pk2(uim[ci][n3][0], uim[ci][n3][1]), pk2(uim[ci][n3][2], uim[ci][n3][3])}; } }
    SVC_SYNC();
    f32x4 gp[NC][2] = {}, gm[NC][2] = {};
    {
        const LAS char* x0 = smem + L_B2X + cl * 256 + ((g ^ (cl & 7)) << 4); const LAS char* x1 = smem + L_B2X + cl * 256 + (((4 + g) ^ (cl & 7)) << 4);
#pragma unroll
        for (int hf = 0; hf < 2; ++hf) {
            bf16x8 ur[NC][2], ui[NC][2];
#pragma unroll
            for (int ci = 0; ci < NC; ++ci)
#pragma unroll
                for (int n4 = 0; n4 < 2; ++n4) { const LAS char* a = (hf ? x1 : x0) + ci * 8192 + n4 * 4096; ur[ci][n4] = ldsr128(a); ui[ci][n4] = ldsr128(a + 128); }
#pragma unroll
            for (int ci = 0; ci < NC; ++ci)
#pragma unroll
                for (int n4 = 0; n4 < 2; ++n4) { gp[ci][n4] = MFMA16(R.A[hf][0], ur[ci][n4], gp[ci][n4]); gm[ci][n4] = MFMA16(R.A[hf][1], ur[ci][n4], gm[ci][n4]); }
#pragma unroll
            for (int ci = 0; ci < NC; ++ci)
#pragma unroll
                for (int n4 = 0; n4 < 2; ++n4) { gp[ci][n4] = MFMA16(R.A[hf][2], ui[ci][n4], gp[ci][n4]); gm[ci][n4] = MFMA16(R.A[hf][0], ui[ci][n4], gm[ci][n4]); }
        }
    }
    LAS char* yw = smem + cb * 2048 + L_Y1S + cl * 144 + (16 * nt + 4 * g) * 2;
#pragma unroll
    for (int ci = 0; ci < NC; ++ci) { const bool special = (jj[ci] == 0 || jj[ci] == 320);
#pragma unroll
        for (int n4 = 0; n4 < 2; ++n4) {
            if (special) gp[ci][n4] = (gp[ci][n4] + gm[ci][n4]) * 0.5f;
            LAS char* d = yw + ci * 10240 + n4 * (16 * 144);
            *(LAS u32x2*)d = (u32x2){pk2(gp[ci][n4][0], gp[ci][n4][1]), pk2(gp[ci][n4][2], gp[ci][n4][3])};
            *(LAS u32x2*)(d + 32 * 144) = (u32x2){pk2(gm[ci][n4][0], gm[ci][n4][1]), pk2(gm[ci][n4][2], gm[ci][n4][3])}; } }
    SVC_SYNC();
    const LAS char* yr = smem + cb * 2048 + L_Y1S + ((nt >> 1) * 32 + 16 * (nt & 1) + (lane >> 3)) * 144 + (lane & 7) * 16;
#pragma unroll
    for (int ci = 0; ci < NC; ++ci) { const int j = jj[ci]; const bool special = (j == 0 || j == 320);
        if (nt < 2 || !special) { char* d = p.ws + WS_Y1 + (size_t)(nt < 2 ? j : 640 - j) * 4096 + (nt & 1) * 2048 + lane * 16;
#pragma unroll
            for (int i = 0; i < 2; ++i) st_sc1_16(d + i * 1024, *(const LAS u32x4*)(yr + ci * 10240 + i * (8 * 144))); } }
}

__global__ __launch_bounds__(768, 3) void mega(Params p) {
    extern __shared__ __attribute__((aligned(16))) char smem_[];
    LAS char* smem = (LAS char*)smem_;
    const unsigned lds0 = (unsigned)(uintptr_t)smem_;
    const int tid = threadIdx.x, lane = tid & 63, w = __builtin_amdgcn_readfirstlane(tid >> 6), b = blockIdx.x;
    const int xcd = b & 7, bi = b >> 3;
    const int gam = (xcd >> 1) * 8 + (bi >> 2), c = (xcd & 1) * 4 + (bi & 3);
    const int wgid = gam * 8 + c;
    ldsw_t C = (ldsw_t)(smem + L_CNT);
    const int role = c & 1, q4 = c >> 1;
#define ST(i)

    const char* kvg = p.ws + WS_KV;
    if (w >= 8) {
        __builtin_amdgcn_s_setprio(2);
        asm volatile("s_barrier" ::: "memory");
        if (w == 8) { if (lane < 32) C[lane] = 0u; else *(ldsw_t)(smem + L_ARR + (lane - 32) * 4) = 0u;
            WAITL0();
            if (lane == 0) { const unsigned long long E = ld_flag(p.ws + WS_EPOCH); const unsigned long long e = E * 0x9E3779B97F4A7C15ull + 0x632BE59BD9B4E019ull;
                C[C_E0] = (unsigned)e; C[C_E1] = (unsigned)(e >> 32); } }
        if (w == 9) { if (lane < 16) C[448 + lane] = 0u;
            for (int u = lane; u < 80; u += 64) *(LAS u32x4*)(smem + L_GW + u * 16) = *(const u32x4*)((const char*)p.gw + u * 16); }
        unsigned long long e;
#define BAR0() do { BAR(); SBAR(); e = (unsigned long long)C[C_E0] | ((unsigned long long)C[C_E1] << 32); ST(1); } while (0)
        if (w >= 10) {
            DhtRegs R; dht_load(R, lane);
            BAR0();
            const int g = lane >> 4, cl = lane & 15, q = cl >> 2, pp = lane & 3, sv = w - 10;
            const unsigned quad = min(sv * 11 + (lane >> 2), 20), k1 = 4 * (wgid & 15) + (lane & 3), gg = (wgid >> 4) & 3;
            const unsigned jq = (wgid >> 6) * 84 + quad * 4, j0 = min(jq, (unsigned)(NJ - 4));
            typedef unsigned u32x2u __attribute__((ext_vector_type(2), aligned(2)));
            const char* pr = (const char*)p.wr + ((k1 + 256 * gg) * NJ + j0) * 2; const char* pi_ = (const char*)p.wi + ((k1 + 256 * gg) * NJ + j0) * 2;
            u32x2 wre[8], wim[8];
#define W_LOADS() do { _Pragma("unroll") for (int i = 0; i < 8; ++i) { const unsigned so = 64 * (16 * (i >> 2) + (i & 3)) * NJ * 2; wre[i] = *(const u32x2u*)(pr + so); wim[i] = *(const u32x2u*)(pi_ + so); } SBAR(); } while (0)
            f32x4 gv = {};
            if (sv == 1) {
                const LAS char* xb = smem + L_X + (16 * q4 + cl) * 128; const int xsw = ((16 * q4 + cl) >> 1) & 7;
                const LAS char* xb0 = xb + ((g ^ xsw) << 4); const LAS char* xb1 = xb + (((4 + g) ^ xsw) << 4); const LAS char* gb = smem + L_GW + 16 * g;
                f32x4 z0 = {}, z1 = {};
#pragma unroll
                for (int ks = 0; ks < 20; ks += 2) { z0 = MFMA16(ldsr128(xb0 + (ks >> 1) * 8192), ldsr128(gb + 64 * ks), z0); z1 = MFMA16(ldsr128(xb1 + (ks >> 1) * 8192), ldsr128(gb + 64 * ks + 64), z1); }
                z0 += z1;
#pragma unroll
                for (int r = 0; r < 4; ++r) gv[r] = __builtin_amdgcn_rcpf(1.0f + __builtin_amdgcn_exp2f(-1.4426950408889634f * z0[r]));
            }
            f32x4 hp[4][2], hm[4][2];
            {
                const LAS char* ad[4][2][2];
#pragma unroll
                for (int rw = 0; rw < 4; ++rw) { const int row = 8 * c + 4 * sv + rw, f = (row >> 1) & 7;
#pragma unroll
                    for (int t = 0; t < 2; ++t) { const int n2 = min(8 * g + 4 * t + q, 19);
#pragma unroll
                        for (int mt = 0; mt < 2; ++mt) ad[rw][mt][t] = smem + L_X + (n2 >> 1) * 8192 + row * 128 + ((((n2 & 1) * 4 + 2 * mt + (pp >> 1)) ^ f) << 4) + (pp & 1) * 8; } }
                dht<4>(ad, R, hp, hm);
            }
            ST(10);
            lds_wait(C, C_F0 + WLOAD, 8u);
            W_LOADS();
            lds_wait(C, C_F0 + 9, 8u);
#pragma unroll
            for (int k = 0; k < 2; ++k) dht_put<16>(smem + L_X1S + 4 * (2 * sv + k), hp[2 * k], hp[2 * k + 1], hm[2 * k], hm[2 * k + 1], 1.0f, 1.0f, lane);
            lds_arrive(C + C_B1); lds_wait(C, C_B1, 2u);
#pragma unroll
            for (int i = 0; i < 5; ++i) { const int P = 5 * sv + i;
                st_sc1_16(p.ws + WS_X1 + (size_t)wgid * 10240 + P * 1024 + lane * 16, *(const LAS u32x4*)(smem + L_X1S + P * 1024 + lane * 16)); }
            if (sv == 1 && cl == 0 && (g >> 1) == (c & 1)) st_sc1_16(p.ws + WS_G + (gam * 64 + 16 * q4 + 4 * g) * 4, __builtin_bit_cast(u32x4, gv));
            ST(11);
            SBAR();
            if (lane < (sv == 0 ? 44 : 40) && jq < NJ) {
#pragma unroll
                for (int cc = 0; cc < 4; ++cc) { unsigned pv[8];
#pragma unroll
                    for (int i = 0; i < 8; ++i) pv[i] = __builtin_amdgcn_perm(wim[i][cc >> 1], wre[i][cc >> 1], (cc & 1) ? 0x07060302u : 0x05040100u);
                    char* d = p.ws + WS_WT + (size_t)(j0 + cc) * 8192 + (k1 >> 4) * 1024 + (gg * 16 + (k1 & 15)) * 16;
                    st_sc1_16(d, (u32x4){pv[0], pv[1], pv[2], pv[3]}); st_sc1_16(d + 4096, (u32x4){pv[4], pv[5], pv[6], pv[7]}); } }
            ST(15);
            WAITV(0);
            if (lds_arrive_ret(C + C_XP) == 1 && lane == 0) st_flag(p.ws + WS_XF + wgid * 128, e);
            ST(2);
        } else BAR0();
        const int s = w - 8, g = lane >> 4, cl = lane & 15;
        if (s < 2) {
            f32x4 a0 = {}, a1 = {};
            {
                const int q = cl >> 2, pp = lane & 3;
                const LAS char* xb = smem + L_X + (16 * q4 + cl) * 128; const int xsw = ((16 * q4 + cl) >> 1) & 7;
                const LAS char* xb0 = xb + ((g ^ xsw) << 4); const LAS char* xb1 = xb + (((4 + g) ^ xsw) << 4);
                const LAS char* wb = smem + L_WKV + (8 * g + q) * 40 + (16 * s + 4 * pp) * 2;
#define SH_BODY(ROLE) do { bf16x8 xf[2][5], wf[2][5]; \
                auto ld = [&](int bt) { _Pragma("unroll") for (int i = 0; i < 5; ++i) { const int ks = 5 * bt + i; \
                    xf[bt & 1][i] = ldsr128(((ks & 1) ? xb1 : xb0) + (ks >> 1) * 8192); wf[bt & 1][i] = cat8(trd(wb + ks * 1280), trd(wb + ks * 1280 + 160)); } }; \
                ld(0); \
                _Pragma("unroll") for (int bt = 0; bt < 4; ++bt) { if (bt < 3) ld(bt + 1); SBAR(); \
                    _Pragma("unroll") for (int i = 0; i < 5; ++i) { \
                        if (i & 1) a1 = ROLE == 0 ? MFMA16(wf[bt & 1][i], xf[bt & 1][i], a1) : MFMA16(xf[bt & 1][i], wf[bt & 1][i], a1); \
                        else a0 = ROLE == 0 ? MFMA16(wf[bt & 1][i], xf[bt & 1][i], a0) : MFMA16(xf[bt & 1][i], wf[bt & 1][i], a0); } \
                    SBAR(); } } while (0)
                if (role == 0) SH_BODY(0); else SH_BODY(1);
            }
            a0 += a1;
            ST(10);
            char* kv = p.ws + WS_KV + (size_t)(2 * gam + (q4 >> 1)) * 3072;
            const unsigned u0 = pk2(a0[0], a0[1]), u1 = pk2(a0[2], a0[3]);
            float mx;
            if (role == 0) {
                const int rho = 16 * (q4 & 1) + cl;
                mx = lo2f(u0) * lo2f(u0) + hi2f(u0) * hi2f(u0) + lo2f(u1) * lo2f(u1) + hi2f(u1) * hi2f(u1);
                if (s == 0) { st_sc1_8(kv + (g & 1) * 512 + rho * 16 + (g >> 1) * 8, (u32x2){u0, u1});
                    mx = sum_xor32(sum_xor16(mx));
                } else { if (g < 2) st_sc1_8(kv + 1024 + g * 256 + rho * 8, g == 0 ? (u32x2){u0, u1} : (u32x2){0x00003f80u, 0u});
                    if (g) mx = 0.f; }
            } else {
                const int sk = q4 & 1, hh = g & 1, jo = g >> 1;
                char* vb = kv + 1536 + (sk * 2 + hh) * 352 + jo * 8;
                mx = fmaxf(fmaxf(fabsf(lo2f(u0)), fabsf(hi2f(u0))), fmaxf(fabsf(lo2f(u1)), fabsf(hi2f(u1))));
                if (!(mx == mx) || lo2f(u0) != lo2f(u0) || hi2f(u0) != hi2f(u0) || lo2f(u1) != lo2f(u1) || hi2f(u1) != hi2f(u1)) mx = INFINITY;
                if (s == 0) st_sc1_8(vb + cl * 16, (u32x2){u0, u1});
                else { if (cl < 6) st_sc1_8(vb + (16 + cl) * 16, cl < 4 ? (u32x2){u0, u1} : cl == 4 ? (u32x2){0x3f803f80u, 0x3f803f80u} : (u32x2){0u, 0u});
                    if (cl >= 4) mx = 0.f; }
            }
            mx = wave_max_nn(mx);
            if (lane == 0) C[C_MX0 + s] = __float_as_uint(mx);
            lds_arrive(C + C_SHR);
            ST(11);
            WAITV(0);
            ST(12);
            if (lds_arrive_ret(C + C_PUB) == 1 && lane == 0) { const float m0 = __uint_as_float(C[C_MX0]), m1 = __uint_as_float(C[C_MX1]);
                st_sc1_16(p.ws + WS_KVF + wgid * KVFP, (u32x4){(unsigned)e, (unsigned)(e >> 32), __float_as_uint(role == 0 ? m0 + m1 : fmaxf(m0, m1)), 0u}); }
            ST(2);
            if (s == 0) {
                const int offs[4] = {lane * KVFP, (lane + 64) * KVFP, (lane + 128) * KVFP, (lane + 192) * KVFP}; unsigned pay[4];
                lds_wait(C, C_F0 + POLL_AFTER, 8u);
                poll_words<4, KVSLEEP>(p.ws + WS_KVF, offs, e, C, pay);
                const float mm = fmaxf(fmaxf(__uint_as_float(pay[0]), __uint_as_float(pay[1])), fmaxf(__uint_as_float(pay[2]), __uint_as_float(pay[3])));
                const int allb = max(max((int)pay[0], (int)pay[1]), max((int)pay[2], (int)pay[3]));
                (void)mm;
                const float km = wave_max_nn((lane & 1) ? 0.f : __int_as_float(allb)), vm = wave_max_nn((lane & 1) ? __int_as_float(allb) : 0.f);
                if (lane == 0) { C[C_KMX] = __float_as_uint(km); C[C_VMX] = __float_as_uint(vm); C[C_KVRDY] = 1u; if (b == 0) st_flag(p.ws + WS_EPOCH, e); }
            } else lds_wait(C, C_KVRDY, 1u);
            ST(13);
#pragma unroll
            for (int i = 0; i < 12; ++i) glds_sc1((unsigned)(s * (OCH * 6144) + i * 1024 + lane * 16), kvg, lds0 + L_KVR + kv_slot(s, i / 6) + (i % 6) * 1024);
            WAITV(0);
            lds_arrive(C + C_KV0);
            ST(3);
        }
        unsigned svc_t = 0;
        {
            FRESH_LANE();
            const bool two = bi < 8 || b == 64;
            const int j0 = xcd * 32 + bi, j1 = bi < 8 ? 256 + xcd * 8 + bi : b == 64 ? 320 : j0;
            B2Regs R;
            const bf16x8* f64 = (const bf16x8*)&B2T.f64[s][0][0][0][0]; const bf16x8* f32 = (const bf16x8*)&B2T.f32[0][0][0][0];
#pragma unroll
            for (int a = 0; a < 2; ++a)
#pragma unroll
                for (int t = 0; t < 3; ++t) { R.A[a][t] = f64[(a * 3 + t) * 64 + lane]; R.F[a][t] = f32[(a * 3 + t) * 64 + lane]; }
#pragma unroll
            for (int i = 0; i < 4; ++i) { R.t1[i] = *(const f32x4*)&B2T.tw1[s][i][lane][0]; R.t2[i] = *(const f32x4*)&B2T.tw2[s][i][lane][0]; }
            if (s == 0) {
                lds_wait(C, C_XP, 2u);
                __builtin_amdgcn_s_sleep(XDLY);
                const int offs[4] = {lane * 128, (lane + 64) * 128, (lane + 128) * 128, (lane + 192) * 128}; unsigned pay[4];
                poll_words<4, 4>(p.ws + WS_XF, offs, e, C, pay);
            }
            else if (s == 3) {
                const unsigned o = (unsigned)(80 * (gam & 7) + lane) * 1280 + 160 * c;
                const unsigned t0 = *(const unsigned*)((const char*)p.wo + o), t1 = *(const unsigned*)((const char*)p.wo + o + 128);
                const unsigned o2 = (unsigned)(80 * (gam & 7) + 64 + (lane & 15)) * 1280 + 160 * c + (lane >> 4 & 1) * 128;
                const unsigned t2 = *(const unsigned*)((const char*)p.wo + o2);
                asm volatile("" :: "v"(t0), "v"(t1), "v"(t2));
            }
            lds_wait(C, C_QX, 8u);
            SVC_SYNC();
            ST(17);
            { const int row = 16 * (s & 1) + (lane >> 3); unsigned vo[2];
#pragma unroll
              for (int i = 0; i < 2; ++i) vo[i] = ((row + 8 * i) * 8 + ((lane & 7) ^ (((row + 8 * i) >> 1) & 7))) * 10240;
#pragma unroll
              for (int ci = 0; ci < 2; ++ci) if (ci == 0 || two) { const int j = ci ? j1 : j0, hr = s < 2 ? j : (640 - j) % 640;
#pragma unroll
                  for (int i = 0; i < 2; ++i) glds_sc1(vo[i], p.ws + WS_X1 + hr * 16, lds0 + L_B2I + ci * 8192 + (2 * s + i) * 1024); } }
            if (s == 0) *(LAS unsigned*)(smem + L_GATE + lane * 4) = __builtin_amdgcn_raw_buffer_load_b32(__builtin_amdgcn_make_buffer_rsrc((void*)(p.ws + WS_G), 0, 8192, 0x00020000), gam * 256 + lane * 4, 0, 16);
            u32x4 wv[2][2];
            wv[0][0] = ld16_sc1(p.ws + WS_WT + (size_t)j0 * 8192, s * 1024 + lane * 16); wv[0][1] = ld16_sc1(p.ws + WS_WT + (size_t)j0 * 8192 + 4096, s * 1024 + lane * 16);
            wv[1][0] = ld16_sc1(p.ws + WS_WT + (size_t)j1 * 8192, s * 1024 + lane * 16); wv[1][1] = ld16_sc1(p.ws + WS_WT + (size_t)j1 * 8192 + 4096, s * 1024 + lane * 16);
            WAITV(0);
            SVC_SYNC();
            ST(19);
#pragma unroll
            for (int i = 0; i < 3; ++i) { const int u = s + 4 * i, k0 = 80 * c + 8 * u;
                if (u < 10) st_sc1_16(p.ws + WS_O + (size_t)((gam * 4 + (lane >> 4)) * 20 + (k0 >> 5)) * 1024 + (((k0 >> 3) & 3) * 16 + (lane & 15)) * 16, (u32x4){~0u, ~0u, ~0u, ~0u}); }

            const int jj[2] = {j0, j1};
            if (two) b2_columns<2>(p, jj, 0, s, lane, smem, C, R, wv, svc_t); else b2_columns<1>(p, jj, 0, s, lane, smem, C, R, wv, svc_t);
            ST(14);
            WAITV(0);
            lds_arrive(C + C_PRE);
            if (lds_arrive_ret(C + C_Y1) == 3 && lane == 0) st_flag(p.ws + WS_YF + wgid * 128, e);
            lds_spin<SVSL>(C, C_Y1, 4u);
            unsigned vo[5];
#pragma unroll
            for (int i = 0; i < 5; ++i) { const int u = 64 * i + lane, row = u / 10, ch = u - row * 10; vo[i] = row * 1280 + 160 * c + ch * 16; }
            for (int a = 0; a < 5; ++a) {
#pragma unroll
                for (int i = 0; i < 5; ++i) glds(vo[i], (const char*)p.wo + (size_t)(5 * s + a) * (32 * 1280), lds0 + L_WO + ((5 * s + a) * 5 + i) * 1024);
                if (a & 1) WAITV(5); }
            WAITV(0);
            lds_arrive(C + C_WO);
            ST(7);
        }
        ST(5);
        u32x2 bv[5];
        {
            FRESH_LANE();
            DhtRegs R; dht_load(R, lane);
            if (s == 0) { const int offs[4] = {lane * 128, (lane + 64) * 128, (lane + 128) * 128, (lane + 192) * 128}; unsigned pay[4];
                { const int o1[1] = {lane < 8 ? (((xcd >> 1) * 8 + (lane >> 2)) * 8 + (xcd & 1) * 4 + (lane & 3)) * 128 : -1}; unsigned p1[1]; poll_words<1, 8>(p.ws + WS_YF, o1, e, C, p1); }
                poll_words<4, 4>(p.ws + WS_YF, offs, e, C, pay);
                lds_arrive(C + C_OOK); }
            SVC_SYNC();
            ST(18);
#pragma unroll
            for (int i = 0; i < 3; ++i) { const int P = s + 4 * i;
                if (P < 10) glds_sc1((unsigned)(lane * 4096), p.ws + WS_Y1 + (size_t)P * (64 * 4096) + gam * 128 + c * 16, lds0 + L_B3 + P * 1024); }
            WAITV(0);
            SVC_SYNC();
            LAS char* rows = smem + L_B3 + s * 2560;
            { unsigned d[10];
#pragma unroll
              for (int i = 0; i < 10; ++i) d[i] = *(const LAS unsigned*)(smem + L_B3 + i * 1024 + lane * 16 + 4 * s);
              SVC_SYNC();
#pragma unroll
              for (int i = 0; i < 10; ++i) { *(LAS u16*)(rows + (64 * i + lane) * 2) = (u16)d[i]; *(LAS u16*)(rows + 1280 + (64 * i + lane) * 2) = (u16)(d[i] >> 16); } }
            WAITL0();
            const int q = cl >> 2, pp = lane & 3;
            const LAS char* ad[2][2][2];
#pragma unroll
            for (int rw = 0; rw < 2; ++rw)
#pragma unroll
                for (int t = 0; t < 2; ++t)
#pragma unroll
                    for (int mt = 0; mt < 2; ++mt) ad[rw][mt][t] = rows + rw * 1280 + min(8 * g + 4 * t + q, 19) * 64 + 32 * mt + 8 * pp;
            f32x4 hp[2][2], hm[2][2];
            dht<2>(ad, R, hp, hm);
            const float s0 = 1.0f - *(const LAS float*)(smem + L_GATE + (8 * c + 2 * s) * 4), s1 = 1.0f - *(const LAS float*)(smem + L_GATE + (8 * c + 2 * s + 1) * 4);
            LAS char* ob = rows;
            dht_put<4>(ob, hp[0], hp[1], hm[0], hm[1], s0, s1, lane);
            WAITL0();
            char* bx = p.ws + WS_BX + (size_t)(gam * 64 + 8 * c + 2 * s) * 1280;
#pragma unroll
            for (int i = 0; i < 2; ++i) if (i == 0 || lane < 16) { const int u = 64 * i + lane;
                const u32x4 d0 = *(const LAS u32x4*)(ob + u * 32), d1 = *(const LAS u32x4*)(ob + u * 32 + 16);
                st_sc1_16(bx + u * 16, (u32x4){__builtin_amdgcn_perm(d0[1], d0[0], 0x05040100u), __builtin_amdgcn_perm(d0[3], d0[2], 0x05040100u), __builtin_amdgcn_perm(d1[1], d1[0], 0x05040100u), __builtin_amdgcn_perm(d1[3], d1[2], 0x05040100u)});
                st_sc1_16(bx + 1280 + u * 16, (u32x4){__builtin_amdgcn_perm(d0[1], d0[0], 0x07060302u), __builtin_amdgcn_perm(d0[3], d0[2], 0x07060302u), __builtin_amdgcn_perm(d1[1], d1[0], 0x07060302u), __builtin_amdgcn_perm(d1[3], d1[2], 0x07060302u)}); }
            WAITV(0);
            if (lds_arrive_ret(C + C_BX) == 3 && lane == 0) st_flag(p.ws + WS_BF + wgid * 128, e);
            ST(6);
            { const int offs[1] = {lane < 8 ? (gam * 8 + lane) * 128 : -1}; unsigned pay[1];
              poll_words<1, 4>(p.ws + WS_BF, offs, e, C, pay); }
            const __amdgpu_buffer_rsrc_t rb = __builtin_amdgcn_make_buffer_rsrc((void*)(p.ws + WS_BX + (size_t)gam * 64 * 1280), 0, 64 * 1280, 0x00020000);
#pragma unroll
            for (int nt = 0; nt < 5; ++nt) { const int tt = nt < 4 ? nt : s, nn = nt < 4 ? s : 4;
                bv[nt] = __builtin_bit_cast(u32x2, __builtin_amdgcn_raw_buffer_load_b64(rb, (16 * tt + cl) * 1280 + (80 * c + 16 * nn + 4 * g) * 2, 0, 16)); }
        }
        {
            FRESH_LANE();
            lds_wait(C, C_T3, 8u);
#pragma unroll
            for (int nt = 0; nt < 5; ++nt) { const int tt = nt < 4 ? nt : s, nn = nt < 4 ? s : 4;
                *(LAS u32x2*)(smem + L_BH + (tt * 5 + nn) * 512 + lane * 8) = bv[nt]; }
            lds_arrive(C + C_BRD);
            ST(8);
        }
    } else {
    unsigned voffs[2]; const char* sb[2]; unsigned sstr[2]; int dsto[2];
        for (int P = w; P < 45; P += 8) {
            if (P < 20) { const int row = 16 * q4 + 8 * (P & 1) + (lane >> 3), ch = lane & 7;
                glds((unsigned)((32 * row + gam) * 1280 + (P >> 1) * 128 + ((ch ^ ((row >> 1) & 7)) << 4)), p.x, lds0 + L_X + (P >> 1) * 8192 + (2 * q4 + (P & 1)) * 1024);
            } else glds((unsigned)((P - 20) * 1024 + lane * 16), role ? p.wv : p.wk, lds0 + L_WKV + (P - 20) * 1024);
        }
        asm volatile("s_barrier" ::: "memory");
#pragma unroll
        for (int i = 0; i < 2; ++i) { const int P = 2 * w + i;
            if (P < 6) { const int pi = P < 2 * q4 ? P : P + 2, row = 8 * pi + (lane >> 3), ch = lane & 7;
                voffs[i] = (32 * row + gam) * 1280 + ((ch ^ ((row >> 1) & 7)) << 4); sb[i] = (const char*)p.x; sstr[i] = 128; dsto[i] = pi * 1024;
            } else { const int u = 64 * (P - 6) + lane, row = u / 10, ch = u - row * 10;
                voffs[i] = row * 1280 + 160 * c + ch * 16; sb[i] = (const char*)p.wq; sstr[i] = 64 * 1280; dsto[i] = (P - 6) * 1024; }
        }
#define FR_ISSUE(kc_) do { _Pragma("unroll") for (int i = 0; i < 2; ++i) \
            glds(voffs[i], sb[i] + (size_t)(kc_) * sstr[i], lds0 + (w < 3 ? L_X + (kc_) * 8192 : wq_slot((kc_) % 5)) + dsto[i]); } while (0)
        FR_ISSUE(0); FR_ISSUE(1); FR_ISSUE(2);
        WAITV(6);
        BAR();
        SBAR();
        const unsigned long long e = (unsigned long long)C[C_E0] | ((unsigned long long)C[C_E1] << 32);
        ST(1);
        const int hh = w & 3, kh = w >> 2, r = lane & 31, h = lane >> 5;
        f32x16 qacc = {};
        {
            const int blk = (lane >> 4) & 1, q = (lane & 15) >> 2, pp = lane & 3;
            const int aoff = (8 * h + q) * 160 + (20 * hh + 16 * blk + 4 * pp) * 2;
            WAITV(4);
            lds_arrive(C + C_F0);
#define FR_WAIT(n) do { switch (n) { case 0: WAITV(0); break; case 2: WAITV(2); break; case 4: WAITV(4); break; case 6: WAITV(6); break; case 8: WAITV(8); break; \
                case 10: WAITV(10); break; case 12: WAITV(12); break; case 14: WAITV(14); break; default: WAITV(16); break; } } while (0)
#pragma unroll
            for (int kc = 0; kc < 10; ++kc) {
                if (kc < 9) { if (kc == 0) WAITV(2); else if (kc == 1) WAITV(0); else if (w < 3) FR_WAIT(2 * (8 - kc)); else FR_WAIT(kc < 8 ? 2 : 0);
                    lds_arrive(C + C_F0 + kc + 1); }
                lds_spin<0>(C, C_F0 + kc, 8u);
                if (kc == 1) { lds_spin<0>(C, C_SHR, 2u);
                    FR_ISSUE(3); FR_ISSUE(4);
                    if (w < 3) { FR_ISSUE(5); FR_ISSUE(6); FR_ISSUE(7); FR_ISSUE(8); FR_ISSUE(9); } }
                if (w >= 3 && kc >= 2 && kc + 3 < 10) FR_ISSUE(kc + 3);
                const LAS char* xs = smem + L_X + kc * 8192; const LAS char* ws_ = smem + wq_slot(kc % 5) + aoff;
#pragma unroll
                for (int ks = 0; ks < 4; ++ks) {
                    const bf16x8 bq = ldsr128(xs + rswz(32 * kh + r, 2 * ks + h));
                    const LAS char* a = ws_ + ks * (16 * 160);
                    qacc = MFMA32(cat8(trd(a), trd(a + 4 * 160)), bq, qacc);
                }
            }
        }
        ST(2);
        u32x4 qa_u[2]; u32x2 qb_u[2];
        {
            const u32x4 qa = {pk2(qacc[0] * QSCALE, qacc[1] * QSCALE), pk2(qacc[2] * QSCALE, qacc[3] * QSCALE), pk2(qacc[4] * QSCALE, qacc[5] * QSCALE), pk2(qacc[6] * QSCALE, qacc[7] * QSCALE)};
            u32x2 qb = {pk2(qacc[8] * QSCALE, qacc[9] * QSCALE), pk2(qacc[10] * QSCALE, qacc[11] * QSCALE)};
            if (h) qb = (u32x2){0u, 0u};
            *(LAS u32x4*)(smem + qx_off(w) + lane * 16) = qa; *(LAS u32x2*)(smem + qx_off(w) + 1024 + lane * 8) = qb;
            lds_arrive(C + C_QX);
            lds_wait(C, C_QX, 8u);
        }
#pragma unroll
        for (int t = 0; t < 2; ++t) { const int ws_ = hh + 4 * t;
            qa_u[t] = *(const LAS u32x4*)(smem + qx_off(ws_) + lane * 16); qb_u[t] = *(const LAS u32x2*)(smem + qx_off(ws_) + 1024 + lane * 8); }
        lds_arrive(C + C_QRD);
        float qq[2];
#pragma unroll
        for (int t = 0; t < 2; ++t) { qq[t] = 0.f;
#pragma unroll
            for (int i = 0; i < 4; ++i) qq[t] += lo2f(qa_u[t][i]) * lo2f(qa_u[t][i]) + hi2f(qa_u[t][i]) * hi2f(qa_u[t][i]);
#pragma unroll
            for (int i = 0; i < 2; ++i) qq[t] += lo2f(qb_u[t][i]) * lo2f(qb_u[t][i]) + hi2f(qb_u[t][i]) * hi2f(qb_u[t][i]);
            qq[t] = sum_xor32(qq[t]); }
        ST(3);
        const int par = w & 1, tl = (w >> 1) & 1;
        const unsigned pvo = kh * (OCH * 6144) + tl * 3072 + lane * 16;
        lds_spin<0>(C, C_QRD, 8u);
        if (par == 0) {
#pragma unroll
            for (int i = 0; i < 3; ++i) glds_sc1(2 * 6144 + pvo + i * 1024, kvg, lds0 + L_KVR + kv_slot(kh, 2) + tl * 3072 + i * 1024); }
        lds_spin<0>(C, C_KV0, 2u);
        const float kmx = __uint_as_float(C[C_KMX]), vmx = __uint_as_float(C[C_VMX]);
        const float mb0 = __builtin_amdgcn_sqrtf(qq[0] * kmx) * 1.01f + 1e-6f, mb1 = __builtin_amdgcn_sqrtf(qq[1] * kmx) * 1.01f + 1e-6f;
        const bool fast = __builtin_amdgcn_readfirstlane((int)(__all(mb0 <= MBOUND && mb1 <= MBOUND) && vmx <= 1.0e9f && (vmx >= 1.0e-9f || vmx == 0.0f))) != 0;
        if (fast && h) { qb_u[0] = (u32x2){pk2(-mb0, 0.f), 0u}; qb_u[1] = (u32x2){pk2(-mb1, 0.f), 0u}; }
        const bf16x8 qaa = __builtin_bit_cast(bf16x8, qa_u[0]), qab = __builtin_bit_cast(bf16x8, qa_u[1]);
        const s16x4 qba = __builtin_bit_cast(s16x4, qb_u[0]), qbb = __builtin_bit_cast(s16x4, qb_u[1]);
        const int koffA = kh * 12288 + h * 512 + r * 16, koffB = kh * 12288 + 1024 + h * 256 + r * 8;
        const int voff_ = kh * 12288 + 1536 + h * 352 + (r <= 20 ? r : 21) * 16;
        ST(4);
        f32x16 oa = {}, ob = {};
        float ma = 0.f, mbb = 0.f;
        if (fast) {
            bf16x8 ka, v0, v1; s16x4 kb; unsigned vpoll, vit, sm0, sc, sspin;
            const unsigned slast = (kh ? (32 - OCH) : OCH) / 4 - 1;
            unsigned voff = (3 + (par ^ 1)) * 6144 + pvo;
            const unsigned ak = lds0 + L_KVR + koffA, akb = lds0 + L_KVR + koffB, av = lds0 + L_KVR + voff_;
            const unsigned awr = lds0 + L_ARR + kh * 64 + (w & 3) * 4, ard = lds0 + L_ARR + kh * 64 + (lane & 3) * 4;
            const unsigned sd0 = __builtin_amdgcn_readfirstlane(lds0 + L_KVR + kh * 12288 + tl * 3072), sd1 = sd0 + 6144, sd2 = sd0 + 24576, sd3 = sd0 + 24576 + 6144;
#define ATTN_CALL(TXT) asm volatile(TXT \
                : [oa] "+v"(oa), [ob] "+v"(ob), [ka] "=&v"(ka), [kb] "=&v"(kb), [v0] "=&v"(v0), [v1] "=&v"(v1), [vpoll] "=&v"(vpoll), [vit] "=&v"(vit), [voff] "+v"(voff), \
                  [sm0] "=&s"(sm0), [sc] "=&s"(sc), [sspin] "=&s"(sspin) \
                : [qaa] "v"(qaa), [qba] "v"(qba), [qab] "v"(qab), [qbb] "v"(qbb), [ak] "v"(ak), [akb] "v"(akb), [av] "v"(av), [awr] "v"(awr), [ard] "v"(ard), \
                  [sg] "s"(kvg), [sd0] "s"(sd0), [sd1] "s"(sd1), [sd2] "s"(sd2), [sd3] "s"(sd3), [slast] "s"(slast) \
                : "memory", "scc", ATTN_ASM_CLOB)
            if (par == 0) ATTN_CALL(ATTN_ASM_TEXT0); else ATTN_CALL(ATTN_ASM_TEXT1);
        } else {
            ma = -INFINITY; mbb = -INFINITY;
            const f32x16 zero16 = {};
            const int nch = kh ? 32 - OCH : OCH;
            for (int ch = 0; ch < nch; ++ch) {
                if (((ch + 1) & 1) == par) WAITV(0);
                WAITL0();
                *(ldsw_t)(smem + L_ARR + kh * 64 + (ch & 3) * 16 + (w & 3) * 4) = (ch >> 2) + 1;
                for (int a = 0; a < 2; ++a) {
                    if (a == 1) { for (unsigned sp = 0; sp < 0x40000u; ++sp) { if (__all(*(ldsw_t)(smem + L_ARR + kh * 64 + (ch & 3) * 16 + (lane & 3) * 4) >= (unsigned)((ch >> 2) + 1))) break; }
                        asm volatile("" ::: "memory");
                        if (ch + 3 < nch && ((ch + 3) & 1) == par) {
#pragma unroll
                            for (int i = 0; i < 3; ++i) glds_sc1((ch + 3) * 6144 + pvo + i * 1024, kvg, lds0 + L_KVR + kv_slot(kh, (ch + 3) & 3) + tl * 3072 + i * 1024); } }
                    const LAS char* tp = smem + L_KVR + kv_slot(0, ch & 3) + a * 3072;
                    const bf16x8 ka = ldsr128(tp + koffA); const s16x4 kb = *(const LAS s16x4*)(tp + koffB);
                    const bf16x8 v0 = ldsr128(tp + voff_), v1 = ldsr128(tp + voff_ + 704);
#pragma unroll
                    for (int t = 0; t < 2; ++t) {
                        f32x16 sc = MFMA32(ka, t ? qab : qaa, zero16); sc = MFMA32K8(kb, t ? qbb : qba, sc);
                        float tm = sc[0];
#pragma unroll
                        for (int i = 1; i < 16; ++i) tm = fmaxf(tm, sc[i]);
                        tm = fmaxf(tm, __shfl_xor(tm, 32));
                        float& m = t ? mbb : ma; f32x16& o = t ? ob : oa;
                        const float mn = fmaxf(m, tm), al = (m == -INFINITY) ? 0.f : __builtin_amdgcn_exp2f(m - mn);
                        m = mn;
                        u32x4 p0, p1;
#pragma unroll
                        for (int i = 0; i < 4; ++i) { p0[i] = pk2(__builtin_amdgcn_exp2f(sc[2 * i] - mn), __builtin_amdgcn_exp2f(sc[2 * i + 1] - mn));
                            p1[i] = pk2(__builtin_amdgcn_exp2f(sc[8 + 2 * i] - mn), __builtin_amdgcn_exp2f(sc[8 + 2 * i + 1] - mn)); }
#pragma unroll
                        for (int i = 0; i < 16; ++i) o[i] *= al;
                        o = MFMA32(v0, __builtin_bit_cast(bf16x8, p0), o);
                        o = MFMA32(v1, __builtin_bit_cast(bf16x8, p1), o);
                    }
                }
            }
        }
        ST(5);
        ST(6);
        {
            const f32x16& og = kh ? oa : ob; const float mg = kh ? ma : mbb;
            LAS char* d = smem + L_KVR + mrg_off(w);
#pragma unroll
            for (int i = 0; i < 3; ++i) *(LAS f32x4*)(d + i * 1024 + lane * 16) = (f32x4){og[4 * i], (i == 2 && h) ? mg : og[4 * i + 1], og[4 * i + 2], og[4 * i + 3]};
        }
        lds_arrive(C + C_T1 + (w & 3));
        lds_spin<0>(C, C_PRE, 4u);
        const int s1 = 32 * kh + r;
        const float gate = *(const LAS float*)(smem + L_GATE + s1 * 4);
        lds_spin<0>(C, C_T1 + (w & 3), 2u);
        {
            const LAS char* sp = smem + L_KVR + mrg_off(w ^ 4);
            const f32x16& ok = kh ? ob : oa;
            float o[12];
#pragma unroll
            for (int i = 0; i < 3; ++i) { const f32x4 v = *(const LAS f32x4*)(sp + i * 1024 + lane * 16);
#pragma unroll
                for (int t = 0; t < 4; ++t) o[4 * i + t] = v[t]; }
            if (fast) {
#pragma unroll
                for (int i = 0; i < 12; ++i) o[i] += ok[i];
            } else {
                const auto mr = __builtin_amdgcn_permlane32_swap(__float_as_uint(o[9]), __float_as_uint(o[9]), false, false);
                const float mk = kh ? mbb : ma, mo = __uint_as_float(mr[1]), mm = fmaxf(mk, mo);
                const float f1 = __builtin_amdgcn_exp2f(mk - mm), f2 = __builtin_amdgcn_exp2f(mo - mm);
#pragma unroll
                for (int i = 0; i < 12; ++i) o[i] = ok[i] * f1 + o[i] * f2;
            }
            const auto lr = __builtin_amdgcn_permlane32_swap(__float_as_uint(o[8]), __float_as_uint(o[8]), false, false);
            const float sc = gate * __builtin_amdgcn_rcpf(__uint_as_float(lr[1]));
            unsigned pk[6];
#pragma unroll
            for (int i = 0; i < 6; ++i) { pk[i] = pk2(o[2 * i] * sc, o[2 * i + 1] * sc); if (pk[i] == ~0u) pk[i] = 0x7fc07fc0u; }
            LAS char* d = smem + L_STG + s1 * 160 + hh * 40;
            *(LAS u32x2*)(d + 8 * h) = (u32x2){pk[0], pk[1]};
            *(LAS u32x2*)(d + 16 + 8 * h) = (u32x2){pk[2], pk[3]};
            if (!h) *(LAS u32x2*)(d + 32) = (u32x2){pk[4], pk[5]};
        }
        lds_arrive(C + C_T3); lds_spin<0>(C, C_T3, 8u);
#pragma unroll
        for (int i = 0; i < 2; ++i) if (i == 0 || w < 2) { const int u = i ? 8 + w : w, k0 = 80 * c + 8 * u;
            const u32x4 v = *(const LAS u32x4*)(smem + L_STG + lane * 160 + u * 16);
            st_sc1_16(p.ws + WS_O + (size_t)((gam * 4 + (lane >> 4)) * 20 + (k0 >> 5)) * 1024 + (((k0 >> 3) & 3) * 16 + (lane & 15)) * 16, v); }
        if (lds_arrive_ret(C + C_T4) == 7 && lane == 0) st_flag(p.ws + WS_OF + wgid * 128, e);
        ST(7);
        {
            const int nt = w & 3;
            const char* ob_ = p.ws + WS_O + (size_t)(gam * 4 + nt) * 20480;
            u32x4 bo[10];
            lds_spin<0>(C, C_OOK, 1u);
            { const int offs[1] = {lane < 4 ? (gam * 8 + 4 * kh + lane) * 128 : -1}; unsigned pay[1];
              poll_words<1, OFSL>(p.ws + WS_OF, offs, e, C, pay); }
#pragma unroll
            for (int i = 0; i < 10; ++i) bo[i] = ld16_sc1(ob_, (10 * kh + i) * 1024 + lane * 16);
            lds_spin<0>(C, C_WO, 4u);
            const int g = lane >> 4, q = (lane & 15) >> 2, pp = lane & 3;
            const LAS char* wb = smem + L_WO + kh * 51200 + (8 * g + q) * 160 + 8 * pp;
            f32x4 acc[5] = {};
            bf16x8 wf[2][5];
#pragma unroll
            for (int n = 0; n < 5; ++n) wf[0][n] = cat8(trd(wb + n * 32), trd(wb + n * 32 + 640));
#pragma unroll
            for (int i = 0; i < 10; ++i) {
                if (i + 1 < 10) {
#pragma unroll
                    for (int n = 0; n < 5; ++n) { const LAS char* a = wb + (i + 1) * 5120 + n * 32; wf[(i + 1) & 1][n] = cat8(trd(a), trd(a + 640)); } }
                for (unsigned sp = 0; sp < CAP_GLB; ++sp) {
                    if (!__any(max(max(bo[i][0], bo[i][1]), max(bo[i][2], bo[i][3])) == ~0u)) break;
                    __builtin_amdgcn_s_sleep(ORSL);
                    bo[i] = ld16_sc1(ob_, (10 * kh + i) * 1024 + lane * 16);
                }
                if (i == 0) { ST(8); }
                SBAR();
#pragma unroll
                for (int n = 0; n < 5; ++n) acc[n] = MFMA16(wf[i & 1][n], __builtin_bit_cast(bf16x8, bo[i]), acc[n]);
                SBAR();
            }
            lds_spin<0>(C, C_T4, 8u);
            LAS char* fw = smem + L_FIN + w * 3072 + lane * 16; const LAS char* fr = smem + L_FIN + (w ^ 4) * 3072 + lane * 16;
            if (kh == 0) { *(LAS f32x4*)fw = acc[3]; *(LAS f32x4*)(fw + 1024) = acc[4]; }
            else { *(LAS f32x4*)fw = acc[0]; *(LAS f32x4*)(fw + 1024) = acc[1]; *(LAS f32x4*)(fw + 2048) = acc[2]; }
            lds_arrive(C + C_T5 + nt);
            lds_spin<0>(C, C_BRD, 4u);
            lds_spin<0>(C, C_T5 + nt, 2u);
            ST(9);
            u16* op = p.out + (size_t)(32 * (16 * nt + (lane & 15)) + gam) * 640 + 80 * c + 4 * g;
            const LAS char* bh = smem + L_BH + nt * 2560 + lane * 8;
#pragma unroll
            for (int i = 0; i < 3; ++i) if (kh == 0 || i < 2) { const int n = kh ? 3 + i : i;
                const u32x2 bb = *(const LAS u32x2*)(bh + n * 512);
                const f32x4 v = (kh ? acc[(3 + i) % 5] : acc[i]) + *(const LAS f32x4*)(fr + i * 1024) + (f32x4){lo2f(bb[0]), hi2f(bb[0]), lo2f(bb[1]), hi2f(bb[1])};
                *(u32x2*)(op + 16 * n) = (u32x2){pk2(v[0], v[1]), pk2(v[2], v[3])}; }
        }
    }
}

extern "C" void kernel_launch(void* const* d_in, const int* in_sizes, int n_in, void* d_out, int out_size, void* d_ws, size_t ws_size, hipStream_t stream) {
    Params p; p.x = (const u16*)d_in[0]; p.wq = (const u16*)d_in[1]; p.wk = (const u16*)d_in[2]; p.wv = (const u16*)d_in[3]; p.wo = (const u16*)d_in[4];
    p.wr = (const u16*)d_in[6]; p.wi = (const u16*)d_in[7]; p.gw = (const u16*)d_in[8]; p.ws = (char*)d_ws; p.out = (u16*)d_out;
    (void)hipFuncSetAttribute((const void*)mega, hipFuncAttributeMaxDynamicSharedMemorySize, L_TOTAL);
    mega<<<256, 768, L_TOTAL, stream>>>(p);
}
```
